```python
import torch

def _softmax(x, axis=-1):
    return torch.softmax(x, dim=axis)

def _gelu(x):
    return torch.nn.functional.gelu(x, approximate='tanh')

def _silu(x):
    return torch.nn.functional.silu(x)

def _sigmoid(x):
    return torch.sigmoid(x)

def _layernorm(x, axis=-1, eps=1e-05):
    m = x.mean(dim=axis, keepdim=True)
    v = x.var(dim=axis, keepdim=True, unbiased=False)
    return (x - m) / torch.sqrt(v + eps)

def _scatter_add(tgt, idx, src, axis=0):
    return tgt.clone().index_add_(axis, idx, src)

def _segment_sum(src, seg, n):
    out = torch.zeros((n,) + src.shape[1:], dtype=src.dtype)
    return out.index_add_(0, seg, src)

def _pad(x, pw):
    flat = tuple((v for pair in reversed(pw) for v in pair))
    return torch.nn.functional.pad(x, flat)
kernel_name = 'standalone__window_attn_davit__vision'

def setup_inputs():

    _w = lambda a: (lambda t: t.to(torch.bfloat16) if t.is_floating_point() else t)(torch.as_tensor(a))
    HW = 512
    C0 = 96
    ws = 7
    nh = 4
    H = W = max(HW // ws * ws, ws)
    C = max(C0 // nh * nh, nh)
    k = 3
    return {'x': _w((lambda *s: torch.randn(*s, dtype=torch.bfloat16) * 1.6)(1, H, W, C)), 'dw_pe': _w((lambda *s: torch.randn(*s, dtype=torch.bfloat16) * 1.6)(k, k, C) * 0.1), 'w_qkv': _w((lambda *s: torch.randn(*s, dtype=torch.bfloat16) * 1.6)(C, 3 * C) * C ** (-0.5)), 'w_o': _w((lambda *s: torch.randn(*s, dtype=torch.bfloat16) * 0.4)(C, C) * C ** (-0.5))}

def reference(x, dw_pe, w_qkv, w_o):
    B, H, W, C = x.shape
    ws = 7
    nh = 4
    dh = C // nh
    kk = dw_pe.shape[0]
    pad = kk // 2
    xp = _pad(x, ((0, 0), (pad, pad), (pad, pad), (0, 0)))
    pe = torch.zeros_like(x)
    for i in range(kk):
        for j in range(kk):
            pe = pe + xp[:, i:i + H, j:j + W, :] * dw_pe[i, j][None, None, None, :]
    x0 = x + pe
    h = _layernorm(x0, axis=-1)
    hw = h.reshape(B, H // ws, ws, W // ws, ws, C)
    hw = (lambda x, *d: x.permute(*d))(hw, 0, 1, 3, 2, 4, 5)
    hw = hw.reshape(-1, ws * ws, C)
    qkv = (hw @ w_qkv).reshape(-1, ws * ws, 3, nh, dh)
    q = qkv[:, :, 0].clone()
    k = qkv[:, :, 1].clone()
    v = qkv[:, :, 2].clone()
    scale = dh ** (-0.5)
    attn = torch.einsum('wnhd,wmhd->whnm', q, k) * scale
    attn = _softmax(attn, axis=-1)
    out = torch.einsum('whnm,wmhd->wnhd', attn, v)
    out = out.reshape(-1, ws * ws, C) @ w_o
    out = out.reshape(B, H // ws, W // ws, ws, ws, C)
    out = (lambda x, *d: x.permute(*d))(out, 0, 1, 3, 2, 4, 5)
    out = out.reshape(B, H, W, C)
    return x0 + out
```

```cpp
#include <hip/hip_runtime.h>
#include <hip/hip_bf16.h>
#include <stdint.h>

typedef __attribute__((ext_vector_type(8))) __bf16 bf16x8;
typedef __attribute__((ext_vector_type(2))) __bf16 bf16x2;
typedef __attribute__((ext_vector_type(4))) float f32x4;
typedef __attribute__((ext_vector_type(2))) float f32x2;
typedef __attribute__((ext_vector_type(4))) unsigned u32x4;
typedef __attribute__((ext_vector_type(2))) unsigned u32x2;
typedef __attribute__((ext_vector_type(4))) short s16x4;
typedef __attribute__((address_space(3))) s16x4* lds_s16x4_p;

#define DEVI __device__ __forceinline__

constexpr int IMG = 511;
constexpr int NWS = 73;
constexpr int NWIN = NWS * NWS;
constexpr int PIXB = 192;
constexpr int ROWB = IMG * PIXB;
constexpr unsigned XBYTES = (unsigned)IMG * IMG * PIXB;
constexpr unsigned OOB = 0x80000000u;

constexpr int XB = 0;
constexpr int RR = 32768;
constexpr int CT = RR + 12288;
constexpr int WO = CT + 30 * 544;
constexpr int LDS_BYTES = WO + 18432;
constexpr int WRAW = 16384;

constexpr int NBLK = 512;

DEVI f32x4 mfma16(u32x4 a, u32x4 b, f32x4 c) {
  return __builtin_amdgcn_mfma_f32_16x16x32_bf16(__builtin_bit_cast(bf16x8, a), __builtin_bit_cast(bf16x8, b), c, 0, 0, 0);
}
DEVI unsigned pk2(float a, float b) {
  bf16x2 v = {(__bf16)a, (__bf16)b};
  return __builtin_bit_cast(unsigned, v);
}
DEVI float bf_lo(unsigned u) { return __builtin_bit_cast(float, u << 16); }
DEVI float bf_hi(unsigned u) { return __builtin_bit_cast(float, u & 0xffff0000u); }

DEVI void dma16(unsigned lds_addr, unsigned voff, u32x4 srd, unsigned soff) {
  asm volatile("s_mov_b32 m0, %0\n\ts_nop 0\n\tbuffer_load_dwordx4 %1, %2, %3 offen lds"
               :: "s"(lds_addr), "v"(voff), "s"(srd), "s"(soff) : "memory");
}
#define BAR_LGKM() asm volatile("s_waitcnt lgkmcnt(0)\n\ts_barrier" ::: "memory")
#define BAR_ALL()  asm volatile("s_waitcnt vmcnt(0) lgkmcnt(0)\n\ts_barrier" ::: "memory")
#define BAR_VM4()  asm volatile("s_waitcnt vmcnt(4) lgkmcnt(0)\n\ts_barrier" ::: "memory")

DEVI u32x4 lds128(const char* smem, int off) { return *(const u32x4*)(smem + off); }

DEVI u32x2 trread(const char* smem, int off) {
  s16x4 v = __builtin_amdgcn_ds_read_tr16_b64_v4i16((lds_s16x4_p)(smem + off));
  return __builtin_bit_cast(u32x2, v);
}

DEVI float red_max_g(float m) {
  unsigned u = __builtin_bit_cast(unsigned, m);
  auto a = __builtin_amdgcn_permlane16_swap(u, u, false, false);
  m = fmaxf(__builtin_bit_cast(float, a[0]), __builtin_bit_cast(float, a[1]));
  u = __builtin_bit_cast(unsigned, m);
  auto b = __builtin_amdgcn_permlane32_swap(u, u, false, false);
  return fmaxf(__builtin_bit_cast(float, b[0]), __builtin_bit_cast(float, b[1]));
}

struct Ctx {
  unsigned xa[5][2];
  unsigned cta;
  unsigned rel[4];
  unsigned orel;
  unsigned l16;
};

__global__ __launch_bounds__(256, 2)
void davit_win_attn_kernel(const unsigned short* __restrict__ x, const unsigned short* __restrict__ dw,
                           const unsigned short* __restrict__ wqkv, const unsigned short* __restrict__ wo,
                           unsigned short* __restrict__ out) {
  __shared__ __attribute__((aligned(16))) char smem[LDS_BYTES];
  const int tid = threadIdx.x;
  const int lane = tid & 63;
  const int w = __builtin_amdgcn_readfirstlane(tid >> 6);
  const int g = lane >> 4, t = lane & 15;
  const unsigned ldsbase = (unsigned)(size_t)smem;

  const int bid = blockIdx.x;
  const int xcd = bid & 7, idx = bid >> 3;
  const int xs = (NWIN * xcd) >> 3, xe = (NWIN * (xcd + 1)) >> 3;
  const int cnt = xe - xs;
  const int basen = cnt >> 6, extra = cnt & 63;
  const int wstart = xs + idx * basen + (idx < extra ? idx : extra);
  const int nwin = basen + (idx < extra ? 1 : 0);

  u32x4 xsrd;
  xsrd[0] = (unsigned)(size_t)x; xsrd[1] = (unsigned)((size_t)x >> 32) & 0xffffu; xsrd[2] = XBYTES; xsrd[3] = 0x00020000u;
  __amdgpu_buffer_rsrc_t osrd = __builtin_amdgcn_make_buffer_rsrc(out, 0, XBYTES, 0x00020000);

  Ctx c;
  c.l16 = lane * 16;
#pragma unroll
  for (int q = 0; q < 4; ++q) {
    unsigned n = (w * 4 + q) * 64 + lane;
    unsigned pos = n / 12u, cc = n - pos * 12u;
    unsigned row = pos / 9u, col = pos - row * 9u;
    unsigned gc = cc ^ (((pos >> 2) & 1u) << 1);
    c.rel[q] = n < 972u ? (row * IMG + col) * PIXB + gc * 16u : OOB;
  }
  auto issue_x = [&](int wi, int buf, bool valid) {
    int wy = wi / NWS, wx = wi - wy * NWS;
    int inb = ((wy * 7 - 1) * IMG + (wx * 7 - 1)) * PIXB;
    bool border = !valid || wy == 0 || wy == NWS - 1 || wx == 0 || wx == NWS - 1;
    unsigned dst = ldsbase + XB + buf * 16384 + w * 4096;
    if (!border) {
#pragma unroll
      for (int q = 0; q < 4; ++q) dma16(dst + q * 1024, c.rel[q], xsrd, (unsigned)inb);
    } else {
#pragma unroll
      for (int q = 0; q < 4; ++q) {
        unsigned n = (w * 4 + q) * 64 + lane;
        unsigned pos = n / 12u;
        unsigned row = pos / 9u, col = pos - row * 9u;
        int gy = wy * 7 - 1 + (int)row, gx = wx * 7 - 1 + (int)col;
        bool ok = valid && n < 972u && gy >= 0 && gy < IMG && gx >= 0 && gx < IMG;
        unsigned vo = ok ? c.rel[q] + (unsigned)inb : OOB;
        dma16(dst + q * 1024, vo, xsrd, 0u);
      }
    }
  };

  issue_x(wstart, 0, true);
  {
    u32x4 wsrd;
    wsrd[0] = (unsigned)(size_t)wqkv; wsrd[1] = (unsigned)((size_t)wqkv >> 32) & 0xffffu; wsrd[2] = 55296u; wsrd[3] = 0x00020000u;
#pragma unroll
    for (int k = 0; k < 14; ++k) {
      int q = k * 4 + w;
      unsigned vo = q < 54 ? (unsigned)(q * 1024 + lane * 16) : OOB;
      if (q < 54) dma16(ldsbase + WRAW + q * 1024, vo, wsrd, 0u);
    }
  }
  u32x4 wor[5];
#pragma unroll
  for (int k = 0; k < 5; ++k) {
    int ch = tid + 256 * k;
    if (ch > 1151) ch = 1151;
    wor[k] = *(const u32x4*)((const char*)wo + ch * 16);
  }
  unsigned short dwv[4];
#pragma unroll
  for (int k = 0; k < 4; ++k) {
    int n = tid + 256 * k;
    if (n > 959) n = 959;
    int rec = n >> 5, b = rec / 5, s = rec - b * 5, hf = (n >> 4) & 1, tt = n & 15;
    int tap = s < 3 ? (hf * 3 + s) : (s == 3 ? 7 : (hf ? 8 : 6));
    unsigned short v = dw[tap * 96 + b * 16 + tt];
    if (s == 3 && hf == 0) v = 0x3f80;
    dwv[k] = v;
  }
  BAR_ALL();

  u32x4 WQ[3], WK[3], WX[3], WV0[3], WV1[3];
  {
    const int cq = t & 3;
    const int rowoff = WRAW + (4 * g + (t >> 2)) * 576;
    const int cQ = (24 * w + 4 * cq) * 2;
    const int cK = (96 + 24 * w + 4 * cq) * 2;
    const int cX = (cq < 2 ? 24 * w + 16 + 4 * cq : 96 + 24 * w + 16 + 4 * (cq - 2)) * 2;
    const int cV = (192 + 24 * w + 4 * cq) * 2;
    const int cV1 = (192 + 24 * w + 16 + 4 * (cq >> 1)) * 2;
    const float qs = 0.20412414523193154f * 1.4426950408889634f;
    const float sx = t < 8 ? qs : 1.0f;
    const bool odd = (t >> 2) & 1;
    const bool keep = (t & 3) < 2;
#pragma unroll
    for (int k = 0; k < 3; ++k) {
#pragma unroll
      for (int jh = 0; jh < 2; ++jh) {
        const int ro = rowoff + (32 * k + 16 * jh) * 576;
        u32x2 q = trread(smem, ro + cQ), kk = trread(smem, ro + cK), xx = trread(smem, ro + cX);
        u32x2 v0 = trread(smem, ro + cV), v1 = trread(smem, ro + cV1);
#pragma unroll
        for (int d = 0; d < 2; ++d) {
          WQ[k][2 * jh + d] = pk2(bf_lo(q[d]) * qs, bf_hi(q[d]) * qs);
          WX[k][2 * jh + d] = pk2(bf_lo(xx[d]) * sx, bf_hi(xx[d]) * sx);
          WK[k][2 * jh + d] = kk[d];
          WV0[k][2 * jh + d] = v0[d];
          unsigned sh = __builtin_amdgcn_update_dpp(0u, v1[d], 0xEE, 0xF, 0xF, true);
          unsigned vv = odd ? sh : v1[d];
          WV1[k][2 * jh + d] = keep ? vv : 0u;
        }
      }
    }
  }
  BAR_LGKM();
#pragma unroll
  for (int k = 0; k < 5; ++k) {
    int ch = tid + 256 * k;
    if (ch < 1152) *(u32x4*)(smem + WRAW + ch * 16) = wor[k];
  }
#pragma unroll
  for (int k = 0; k < 4; ++k) {
    int n = tid + 256 * k;
    if (n < 960) {
      int rec = n >> 5, hf = (n >> 4) & 1, tt = n & 15;
      int i8 = (tt & 3) + 4 * (tt >> 3);
      int slot = (tt & 4) ? 9 + 16 * hf + i8 : 2 + 16 * hf + (i8 < 7 ? i8 : 15);
      unsigned val = (unsigned)dwv[k] << ((tt & 1) * 16);
      int d = (tt & 7) >> 1;
      u32x4 e;
      e[0] = d == 0 ? val : 0u; e[1] = d == 1 ? val : 0u; e[2] = d == 2 ? val : 0u; e[3] = d == 3 ? val : 0u;
      *(u32x4*)(smem + CT + rec * 544 + slot * 16) = e;
    }
  }
  if (tid < 60) {
    u32x4 z = {0u, 0u, 0u, 0u};
    *(u32x4*)(smem + CT + (tid >> 1) * 544 + (tid & 1) * 16) = z;
  }
  BAR_LGKM();
  for (int bp = w; bp < 6; bp += 4) {
    const int base = WRAW + (24 * g + (t >> 2)) * 192 + (16 * bp + 4 * (t & 3)) * 2;
    u32x4 f0, f1, f2;
#pragma unroll
    for (int gs = 0; gs < 4; ++gs) {
      u32x2 a = trread(smem, base + 4 * gs * 192);
      f0[gs] = a[0]; f1[gs] = a[1];
    }
#pragma unroll
    for (int u = 0; u < 2; ++u) {
      u32x2 a = trread(smem, base + (16 + 4 * u) * 192);
      f2[2 * u] = a[0]; f2[2 * u + 1] = a[1];
    }
    *(u32x4*)(smem + WO + (bp * 3 + 0) * 1024 + lane * 16) = f0;
    *(u32x4*)(smem + WO + (bp * 3 + 1) * 1024 + lane * 16) = f1;
    *(u32x4*)(smem + WO + (bp * 3 + 2) * 1024 + lane * 16) = f2;
  }
  {
    int set = 2 * w + ((t >= 4 && t < 12) ? 1 : 0);
    int r = t < 4 ? t : (t < 12 ? t - 4 : t - 8);
    int ty = set + (set >= ((r + 1) & 7) ? 1 : 0);
    int tx = (r - ty) & 7;
    if (w == 3) { ty = 6; tx = 0; }
    const int pos = 9 * (ty + 1) + (tx + 1);
    const int hf = g >> 1, e = g & 1;
    const int d0[5] = {-10, -9, -8, 0, 8};
    const int d1[5] = {-1, 0, 1, 9, 10};
#pragma unroll
    for (int s = 0; s < 5; ++s) {
      int p = pos + (hf ? d1[s] : d0[s]);
      unsigned a = XB + p * 192 + e * 16;
      unsigned sw = ((p >> 2) & 1) * 32;
      c.xa[s][0] = a + sw;
      c.xa[s][1] = a + (sw ^ 32u);
    }
    int i8 = (t & 3) + 4 * (t >> 3);
    int slot = (t & 4) ? 9 + 16 * hf + i8 : 2 + 16 * hf + (i8 < 7 ? i8 : 15);
    bool act = e == (t >> 3);
    c.cta = CT + (act ? slot : ((t & 4) ? 0 : 1)) * 16;
    c.orel = (ty * IMG + tx) * PIXB + g * 8;
    if (w == 3 && t != 0) c.orel = OOB;
  }
  BAR_LGKM();
  issue_x(wstart + 1, 1, nwin > 1);

  const u32x4 ONES = {0x3f803f80u, 0x3f803f80u, 0x3f803f80u, 0x3f803f80u};
  const f32x4 ZERO4 = {0.f, 0.f, 0.f, 0.f};
  f32x4 cV1, cS3;
  {
    float o = (t & 3) == 2 ? 1.0f : 0.0f;
    cV1 = f32x4{o, o, o, o};
    cS3 = f32x4{g == 0 ? 0.0f : -INFINITY, 0.f, 0.f, 0.f};
  }
  f32x4 x0[6];
  unsigned obase_prev = 0;

  auto phase3 = [&](unsigned obase) {
    u32x4 of[3];
#pragma unroll
    for (int k = 0; k < 3; ++k) of[k] = lds128(smem, RR + w * 3072 + k * 1024 + c.l16);
#pragma unroll
    for (int bp = 0; bp < 6; ++bp) {
      f32x4 acc = x0[bp];
#pragma unroll
      for (int k = 0; k < 3; ++k) acc = mfma16(lds128(smem, WO + (bp * 3 + k) * 1024 + c.l16), of[k], acc);
      u32x2 o = {pk2(acc[0], acc[1]), pk2(acc[2], acc[3])};
      __builtin_amdgcn_raw_buffer_store_b64(o, osrd, c.orel + bp * 32, obase, 0);
    }
  };

  auto window = [&](auto BUFC, int i) {
    constexpr int BUF = decltype(BUFC)::value;
    const int wi = wstart + i;
    const int wy = wi / NWS, wx = wi - wy * NWS;
    const unsigned obase = (unsigned)((wy * 7 * IMG + wx * 7) * PIXB);
    if (i > 0) phase3(obase_prev);
    obase_prev = obase;
#pragma unroll
    for (int b = 0; b < 6; ++b) x0[b] = ZERO4;
#pragma unroll
    for (int s = 0; s < 5; ++s)
#pragma unroll
      for (int b = 0; b < 6; ++b)
        x0[b] = mfma16(lds128(smem, c.cta + (b * 5 + s) * 544),
                       lds128(smem, c.xa[s][b & 1] + BUF * 16384 + 64 * (b >> 1)), x0[b]);
    {
      u32x4 xb[3];
#pragma unroll
      for (int k = 0; k < 3; ++k)
        xb[k] = u32x4{pk2(x0[2 * k][0], x0[2 * k][1]), pk2(x0[2 * k][2], x0[2 * k][3]),
                      pk2(x0[2 * k + 1][0], x0[2 * k + 1][1]), pk2(x0[2 * k + 1][2], x0[2 * k + 1][3])};
      f32x4 s1 = ZERO4, s2 = ZERO4;
#pragma unroll
      for (int k = 0; k < 3; ++k) { s1 = mfma16(ONES, xb[k], s1); s2 = mfma16(xb[k], xb[k], s2); }
      const bool dg = g == (t >> 2);
      float d = (t & 3) == 0 ? s2[0] : ((t & 3) == 1 ? s2[1] : ((t & 3) == 2 ? s2[2] : s2[3]));
      d = dg ? d : 0.0f;
      f32x4 sq = __builtin_amdgcn_mfma_f32_16x16x4f32(1.0f, d, ZERO4, 0, 0, 0);
      float mean = s1[0] * (1.0f / 96.0f);
      float var = fmaxf(sq[0] * (1.0f / 96.0f) - mean * mean, 0.0f);
      float rstd = __builtin_amdgcn_rsqf(var + 1e-5f);
      float nm = -mean * rstd;
#pragma unroll
      for (int k = 0; k < 3; ++k) {
        u32x4 hv;
#pragma unroll
        for (int u = 0; u < 2; ++u) {
          hv[2 * u] = pk2(fmaf(x0[2 * k + u][0], rstd, nm), fmaf(x0[2 * k + u][1], rstd, nm));
          hv[2 * u + 1] = pk2(fmaf(x0[2 * k + u][2], rstd, nm), fmaf(x0[2 * k + u][3], rstd, nm));
        }
        *(u32x4*)(smem + RR + w * 3072 + k * 1024 + c.l16) = hv;
      }
    }
    BAR_LGKM();
    issue_x(wi + 2, BUF, i + 2 < nwin);
    u32x4 hf[4][3];
#pragma unroll
    for (int T = 0; T < 4; ++T)
#pragma unroll
      for (int k = 0; k < 3; ++k) hf[T][k] = lds128(smem, RR + T * 3072 + k * 1024 + c.l16);
    BAR_LGKM();
    u32x4 Qf[4], Kf[4];
    u32x2 V0p[4], V1p[4];
#pragma unroll
    for (int T = 0; T < 4; ++T) {
      f32x4 q = ZERO4, kk = ZERO4, xx = ZERO4, v0 = ZERO4, v1 = cV1;
#pragma unroll
      for (int k = 0; k < 3; ++k) {
        q = mfma16(WQ[k], hf[T][k], q);
        kk = mfma16(WK[k], hf[T][k], kk);
        xx = mfma16(WX[k], hf[T][k], xx);
        v0 = mfma16(hf[T][k], WV0[k], v0);
        v1 = mfma16(hf[T][k], WV1[k], v1);
      }
      auto sw = __builtin_amdgcn_permlane32_swap(pk2(xx[0], xx[1]), pk2(xx[2], xx[3]), false, false);
      Qf[T] = u32x4{pk2(q[0], q[1]), pk2(q[2], q[3]), sw[0], 0u};
      Kf[T] = u32x4{pk2(kk[0], kk[1]), pk2(kk[2], kk[3]), sw[1], 0u};
      V0p[T] = u32x2{pk2(v0[0], v0[1]), pk2(v0[2], v0[3])};
      V1p[T] = u32x2{pk2(v1[0], v1[1]), pk2(v1[2], v1[3])};
    }
    const u32x4 VA0 = {V0p[0][0], V0p[0][1], V0p[1][0], V0p[1][1]};
    const u32x4 VA1 = {V0p[2][0], V0p[2][1], V0p[3][0], V0p[3][1]};
    const u32x4 VB0 = {V1p[0][0], V1p[0][1], V1p[1][0], V1p[1][1]};
    const u32x4 VB1 = {V1p[2][0], V1p[2][1], V1p[3][0], V1p[3][1]};
#pragma unroll
    for (int T = 0; T < 4; ++T) {
      f32x4 s0 = mfma16(Kf[0], Qf[T], ZERO4);
      f32x4 s1 = mfma16(Kf[1], Qf[T], ZERO4);
      f32x4 s2 = mfma16(Kf[2], Qf[T], ZERO4);
      f32x4 s3 = mfma16(Kf[3], Qf[T], cS3);
      float m = fmaxf(fmaxf(fmaxf(s0[0], s0[1]), fmaxf(s0[2], s0[3])), fmaxf(fmaxf(s1[0], s1[1]), fmaxf(s1[2], s1[3])));
      m = fmaxf(m, fmaxf(fmaxf(fmaxf(s2[0], s2[1]), fmaxf(s2[2], s2[3])), s3[0]));
      m = red_max_g(m);
      u32x4 p0, p1;
      p0[0] = pk2(__builtin_amdgcn_exp2f(s0[0] - m), __builtin_amdgcn_exp2f(s0[1] - m));
      p0[1] = pk2(__builtin_amdgcn_exp2f(s0[2] - m), __builtin_amdgcn_exp2f(s0[3] - m));
      p0[2] = pk2(__builtin_amdgcn_exp2f(s1[0] - m), __builtin_amdgcn_exp2f(s1[1] - m));
      p0[3] = pk2(__builtin_amdgcn_exp2f(s1[2] - m), __builtin_amdgcn_exp2f(s1[3] - m));
      p1[0] = pk2(__builtin_amdgcn_exp2f(s2[0] - m), __builtin_amdgcn_exp2f(s2[1] - m));
      p1[1] = pk2(__builtin_amdgcn_exp2f(s2[2] - m), __builtin_amdgcn_exp2f(s2[3] - m));
      p1[2] = pk2(__builtin_amdgcn_exp2f(s3[0] - m), 0.0f);
      p1[3] = 0u;
      f32x4 o0 = mfma16(VA0, p0, ZERO4);
      f32x4 o1 = mfma16(VB0, p0, ZERO4);
      o0 = mfma16(VA1, p1, o0);
      o1 = mfma16(VB1, p1, o1);
      float rl = __builtin_amdgcn_rcpf(o1[2]);
      char* op = smem + RR + T * 3072 + w * 256 + t * 16 + g * 4;
      *(unsigned*)(op) = pk2(o0[0] * rl, o0[1] * rl);
      *(unsigned*)(op + 1024) = pk2(o0[2] * rl, o0[3] * rl);
      *(unsigned*)(op + 2048) = pk2(o1[0] * rl, o1[1] * rl);
    }
    BAR_VM4();
  };

  struct B0 { enum { value = 0 }; };
  struct B1 { enum { value = 1 }; };
  for (int i = 0; i < nwin; i += 2) {
    window(B0{}, i);
    if (i + 1 >= nwin) break;
    window(B1{}, i + 1);
  }
  phase3(obase_prev);
}

extern "C" void kernel_launch(void* const* d_in, const int* in_sizes, int n_in,
                              void* d_out, int out_size, void* d_ws, size_t ws_size,
                              hipStream_t stream) {
  const unsigned short* x = (const unsigned short*)d_in[0];
  const unsigned short* dw = (const unsigned short*)d_in[1];
  const unsigned short* wqkv = (const unsigned short*)d_in[2];
  const unsigned short* wo = (const unsigned short*)d_in[3];
  davit_win_attn_kernel<<<NBLK, 256, 0, stream>>>(x, dw, wqkv, wo, (unsigned short*)d_out);
}
```

```cpp
#include <hip/hip_runtime.h>
#include <hip/hip_bf16.h>
#include <stdint.h>

typedef __attribute__((ext_vector_type(8))) __bf16 bf16x8;
typedef __attribute__((ext_vector_type(2))) __bf16 bf16x2;
typedef __attribute__((ext_vector_type(4))) float f32x4;
typedef __attribute__((ext_vector_type(2))) float f32x2;
typedef __attribute__((ext_vector_type(4))) unsigned u32x4;
typedef __attribute__((ext_vector_type(2))) unsigned u32x2;
typedef __attribute__((ext_vector_type(4))) short s16x4;
typedef __attribute__((address_space(3))) s16x4* lds_s16x4_p;

#define DEVI __device__ __forceinline__

constexpr int IMG = 511;
constexpr int NWS = 73;
constexpr int NWIN = NWS * NWS;
constexpr int PIXB = 192;
constexpr int ROWB = IMG * PIXB;
constexpr unsigned XBYTES = (unsigned)IMG * IMG * PIXB;
constexpr unsigned OOB = 0x80000000u;

constexpr int XB = 0;
constexpr int RR = 32768;
constexpr int CT = RR + 12288;
constexpr int WO = CT + 30 * 544;
constexpr int PS = WO + 18432;
constexpr int LDS_BYTES = PS + 512;
constexpr int WRAW = 16384;

constexpr int NBLK = 512;

DEVI f32x4 mfma16(u32x4 a, u32x4 b, f32x4 c) {
  return __builtin_amdgcn_mfma_f32_16x16x32_bf16(__builtin_bit_cast(bf16x8, a), __builtin_bit_cast(bf16x8, b), c, 0, 0, 0);
}
DEVI unsigned pk2(float a, float b) {
  bf16x2 v = {(__bf16)a, (__bf16)b};
  return __builtin_bit_cast(unsigned, v);
}
DEVI unsigned fbits(float f) { return __builtin_bit_cast(unsigned, f); }
DEVI float bf_lo(unsigned u) { return __builtin_bit_cast(float, u << 16); }
DEVI float bf_hi(unsigned u) { return __builtin_bit_cast(float, u & 0xffff0000u); }

DEVI void dma16(unsigned lds_addr, unsigned voff, u32x4 srd, unsigned soff) {
  asm volatile("s_mov_b32 m0, %0\n\ts_nop 0\n\tbuffer_load_dwordx4 %1, %2, %3 offen lds"
               :: "s"(lds_addr), "v"(voff), "s"(srd), "s"(soff) : "memory");
}
#define BAR_LGKM() asm volatile("s_waitcnt lgkmcnt(0)\n\ts_barrier" ::: "memory")
#define BAR_ALL()  asm volatile("s_waitcnt vmcnt(0) lgkmcnt(0)\n\ts_barrier" ::: "memory")
#define BAR_VM4()  asm volatile("s_waitcnt vmcnt(4) lgkmcnt(0)\n\ts_barrier" ::: "memory")

DEVI u32x4 lds128(const char* smem, int off) { return *(const u32x4*)(smem + off); }

DEVI u32x2 trread(const char* smem, int off) {
  s16x4 v = __builtin_amdgcn_ds_read_tr16_b64_v4i16((lds_s16x4_p)(smem + off));
  return __builtin_bit_cast(u32x2, v);
}

#define STAMP(k) do {} while (0)

struct Ctx {
  unsigned xa[5][2];
  unsigned cta;
  unsigned rel[4];
  unsigned orel;
  unsigned l16;
};

__global__ __launch_bounds__(256, 2)
void davit_win_attn_kernel(const unsigned short* __restrict__ x, const unsigned short* __restrict__ dw,
                           const unsigned short* __restrict__ wqkv, const unsigned short* __restrict__ wo,
                           unsigned short* __restrict__ out
                           ) {
  __shared__ __attribute__((aligned(16))) char smem[LDS_BYTES];
  const int tid = threadIdx.x;
  const int lane = tid & 63;
  const int w = __builtin_amdgcn_readfirstlane(tid >> 6);
  const int g = lane >> 4, t = lane & 15;
  const unsigned ldsbase = (unsigned)(size_t)smem;

  const int bid = blockIdx.x;
  const int xcd = bid & 7, idx = bid >> 3;
  const int xs = (NWIN * xcd) >> 3, xe = (NWIN * (xcd + 1)) >> 3;
  const int cnt = xe - xs;
  const int basen = cnt >> 6, extra = cnt & 63;
  const int wstart = xs + idx * basen + (idx < extra ? idx : extra);
  const int nwin = basen + (idx < extra ? 1 : 0);

  u32x4 xsrd;
  xsrd[0] = (unsigned)(size_t)x; xsrd[1] = (unsigned)((size_t)x >> 32) & 0xffffu; xsrd[2] = XBYTES; xsrd[3] = 0x00020000u;
  __amdgpu_buffer_rsrc_t osrd = __builtin_amdgcn_make_buffer_rsrc(out, 0, XBYTES, 0x00020000);

  Ctx c;
  c.l16 = lane * 16;
#pragma unroll
  for (int q = 0; q < 4; ++q) {
    unsigned n = (w * 4 + q) * 64 + lane;
    unsigned pos = n / 12u, cc = n - pos * 12u;
    unsigned row = pos / 9u, col = pos - row * 9u;
    unsigned gc = cc ^ (((pos >> 2) & 1u) << 1);
    c.rel[q] = n < 972u ? (row * IMG + col) * PIXB + gc * 16u : OOB;
  }
  auto issue_x = [&](int wi, int buf, bool valid) {
    int wy = wi / NWS, wx = wi - wy * NWS;
    int inb = ((wy * 7 - 1) * IMG + (wx * 7 - 1)) * PIXB;
    bool border = !valid || wy == 0 || wy == NWS - 1 || wx == 0 || wx == NWS - 1;
    unsigned dst = ldsbase + XB + buf * 16384 + w * 4096;
    if (!border) {
#pragma unroll
      for (int q = 0; q < 4; ++q) dma16(dst + q * 1024, c.rel[q], xsrd, (unsigned)inb);
    } else {
#pragma unroll
      for (int q = 0; q < 4; ++q) {
        unsigned n = (w * 4 + q) * 64 + lane;
        unsigned pos = n / 12u;
        unsigned row = pos / 9u, col = pos - row * 9u;
        int gy = wy * 7 - 1 + (int)row, gx = wx * 7 - 1 + (int)col;
        bool ok = valid && n < 972u && gy >= 0 && gy < IMG && gx >= 0 && gx < IMG;
        unsigned vo = ok ? c.rel[q] + (unsigned)inb : OOB;
        dma16(dst + q * 1024, vo, xsrd, 0u);
      }
    }
  };

  issue_x(wstart, 0, true);
  {
    u32x4 wsrd;
    wsrd[0] = (unsigned)(size_t)wqkv; wsrd[1] = (unsigned)((size_t)wqkv >> 32) & 0xffffu; wsrd[2] = 55296u; wsrd[3] = 0x00020000u;
#pragma unroll
    for (int k = 0; k < 14; ++k) {
      int q = k * 4 + w;
      unsigned vo = q < 54 ? (unsigned)(q * 1024 + lane * 16) : OOB;
      if (q < 54) dma16(ldsbase + WRAW + q * 1024, vo, wsrd, 0u);
    }
  }
  u32x4 wor[5];
#pragma unroll
  for (int k = 0; k < 5; ++k) {
    int ch = tid + 256 * k;
    if (ch > 1151) ch = 1151;
    wor[k] = *(const u32x4*)((const char*)wo + ch * 16);
  }
  unsigned short dwv[4];
#pragma unroll
  for (int k = 0; k < 4; ++k) {
    int n = tid + 256 * k;
    if (n > 959) n = 959;
    int rec = n >> 5, b = rec / 5, s = rec - b * 5, hf = (n >> 4) & 1, tt = n & 15;
    int tap = s < 3 ? (hf * 3 + s) : (s == 3 ? 7 : (hf ? 8 : 6));
    unsigned short v = dw[tap * 96 + b * 16 + tt];
    if (s == 3 && hf == 0) v = 0x3f80;
    dwv[k] = v;
  }
  BAR_ALL();

  u32x4 WQ[3], WK[3], WX[3], WV0[3], WV1[3];
  {
    const int cq = t & 3;
    const int rowoff = WRAW + (4 * g + (t >> 2)) * 576;
    const int cQ = (24 * w + 4 * cq) * 2;
    const int cK = (96 + 24 * w + 4 * cq) * 2;
    const int cX = (cq < 2 ? 24 * w + 16 + 4 * cq : 96 + 24 * w + 16 + 4 * (cq - 2)) * 2;
    const int cV = (192 + 24 * w + 4 * cq) * 2;
    const int cV1 = (192 + 24 * w + 16 + 4 * (cq >> 1)) * 2;
    const float qs = 0.20412414523193154f * 1.4426950408889634f;
    const float sx = t < 8 ? qs : 1.0f;
    const bool odd = (t >> 2) & 1;
    const bool keep = (t & 3) < 2;
#pragma unroll
    for (int k = 0; k < 3; ++k) {
#pragma unroll
      for (int jh = 0; jh < 2; ++jh) {
        const int ro = rowoff + (32 * k + 16 * jh) * 576;
        u32x2 q = trread(smem, ro + cQ), kk = trread(smem, ro + cK), xx = trread(smem, ro + cX);
        u32x2 v0 = trread(smem, ro + cV), v1 = trread(smem, ro + cV1);
#pragma unroll
        for (int d = 0; d < 2; ++d) {
          WQ[k][2 * jh + d] = pk2(bf_lo(q[d]) * qs, bf_hi(q[d]) * qs);
          WX[k][2 * jh + d] = pk2(bf_lo(xx[d]) * sx, bf_hi(xx[d]) * sx);
          WK[k][2 * jh + d] = kk[d];
          WV0[k][2 * jh + d] = v0[d];
          unsigned sh = __builtin_amdgcn_update_dpp(0u, v1[d], 0xEE, 0xF, 0xF, true);
          unsigned vv = odd ? sh : v1[d];
          WV1[k][2 * jh + d] = keep ? vv : 0u;
        }
      }
    }
  }
  bool force_safe;
  {
    unsigned mx = 0;
#pragma unroll
    for (int k = 0; k < 3; ++k)
#pragma unroll
      for (int d = 0; d < 4; ++d) {
        mx = max(mx, max(WV0[k][d] & 0x7fff0000u, (WV0[k][d] & 0x7fffu) << 16));
        mx = max(mx, max(WV1[k][d] & 0x7fff0000u, (WV1[k][d] & 0x7fffu) << 16));
      }
    force_safe = __builtin_amdgcn_ballot_w64(mx >= 0x5d800000u) != 0ull;
  }
  BAR_LGKM();
#pragma unroll
  for (int k = 0; k < 5; ++k) {
    int ch = tid + 256 * k;
    if (ch < 1152) *(u32x4*)(smem + WRAW + ch * 16) = wor[k];
  }
#pragma unroll
  for (int k = 0; k < 4; ++k) {
    int n = tid + 256 * k;
    if (n < 960) {
      int rec = n >> 5, hf = (n >> 4) & 1, tt = n & 15;
      int i8 = (tt & 3) + 4 * (tt >> 3);
      int slot = (tt & 4) ? 9 + 16 * hf + i8 : 2 + 16 * hf + (i8 < 7 ? i8 : 15);
      unsigned val = (unsigned)dwv[k] << ((tt & 1) * 16);
      int d = (tt & 7) >> 1;
      u32x4 e;
      e[0] = d == 0 ? val : 0u; e[1] = d == 1 ? val : 0u; e[2] = d == 2 ? val : 0u; e[3] = d == 3 ? val : 0u;
      *(u32x4*)(smem + CT + rec * 544 + slot * 16) = e;
    }
  }
  if (tid < 60) {
    u32x4 z = {0u, 0u, 0u, 0u};
    *(u32x4*)(smem + CT + (tid >> 1) * 544 + (tid & 1) * 16) = z;
  }
  BAR_LGKM();
  for (int bp = w; bp < 6; bp += 4) {
    const int base = WRAW + (24 * g + (t >> 2)) * 192 + (16 * bp + 4 * (t & 3)) * 2;
    u32x4 f0, f1, f2;
#pragma unroll
    for (int gs = 0; gs < 4; ++gs) {
      u32x2 a = trread(smem, base + 4 * gs * 192);
      f0[gs] = a[0]; f1[gs] = a[1];
    }
#pragma unroll
    for (int u = 0; u < 2; ++u) {
      u32x2 a = trread(smem, base + (16 + 4 * u) * 192);
      f2[2 * u] = a[0]; f2[2 * u + 1] = a[1];
    }
    *(u32x4*)(smem + WO + (bp * 3 + 0) * 1024 + lane * 16) = f0;
    *(u32x4*)(smem + WO + (bp * 3 + 1) * 1024 + lane * 16) = f1;
    *(u32x4*)(smem + WO + (bp * 3 + 2) * 1024 + lane * 16) = f2;
  }
  {
    int set = 2 * w + ((t >= 4 && t < 12) ? 1 : 0);
    int r = t < 4 ? t : (t < 12 ? t - 4 : t - 8);
    int ty = set + (set >= ((r + 1) & 7) ? 1 : 0);
    int tx = (r - ty) & 7;
    if (w == 3) { ty = 6; tx = 0; }
    const int pos = 9 * (ty + 1) + (tx + 1);
    const int hf = g >> 1, e = g & 1;
    const int d0[5] = {-10, -9, -8, 0, 8};
    const int d1[5] = {-1, 0, 1, 9, 10};
#pragma unroll
    for (int s = 0; s < 5; ++s) {
      int p = pos + (hf ? d1[s] : d0[s]);
      unsigned a = XB + p * 192 + e * 16;
      unsigned sw = ((p >> 2) & 1) * 32;
      c.xa[s][0] = a + sw;
      c.xa[s][1] = a + (sw ^ 32u);
    }
    int i8 = (t & 3) + 4 * (t >> 3);
    int slot = (t & 4) ? 9 + 16 * hf + i8 : 2 + 16 * hf + (i8 < 7 ? i8 : 15);
    bool act = e == (t >> 3);
    c.cta = CT + (act ? slot : ((t & 4) ? 0 : 1)) * 16;
    c.orel = (ty * IMG + tx) * PIXB + g * 8;
    if (w == 3 && t != 0) c.orel = OOB;
  }
  BAR_LGKM();
  issue_x(wstart + 1, 1, nwin > 1);
  STAMP(0);

  const u32x4 ONES = {0x3f803f80u, 0x3f803f80u, 0x3f803f80u, 0x3f803f80u};
  const f32x4 ZERO4 = {0.f, 0.f, 0.f, 0.f};
  f32x4 cV1, cS3;
  {
    float o = (t & 3) == 2 ? 1.0f : 0.0f;
    cV1 = f32x4{o, o, o, o};
    cS3 = f32x4{g == 0 ? 0.0f : -INFINITY, t == 0 ? 0.0f : -INFINITY, 0.f, 0.f};
  }
  const bool dm0 = lane == 0 + 0 || lane == 16 + 4 || lane == 32 + 8 || lane == 48 + 12;
  const bool dm1 = lane == 0 + 1 || lane == 16 + 5 || lane == 32 + 9 || lane == 48 + 13;
  const bool dm2 = lane == 0 + 2 || lane == 16 + 6 || lane == 32 + 10 || lane == 48 + 14;
  const bool dm3 = lane == 0 + 3 || lane == 16 + 7 || lane == 32 + 11 || lane == 48 + 15;
  const bool g0 = g == 0, g1 = g == 1, g2 = g == 2;
  const unsigned ow = RR + w * 256 + t * 16 + g * 4;
  const unsigned psw = PS + w * 128 + (g >> 1) * 64 + (t >> 2) * 16 + ((g & 1) * 4 + (t & 3)) * 2;
  const unsigned psr = PS + w * 128 + g * 16;
  const unsigned rw = RR + w * 3072 + c.l16;
  f32x4 x0[6];
#pragma unroll
  for (int b = 0; b < 6; ++b) x0[b] = ZERO4;
  unsigned obase_prev = 0;

  unsigned wob = WO + c.l16;
  asm volatile("" : "+v"(wob));
  auto phase3 = [&](unsigned obase, __amdgpu_buffer_rsrc_t srd) {
    u32x4 of[3];
#pragma unroll
    for (int k = 0; k < 3; ++k) of[k] = lds128(smem, rw + k * 1024);
#pragma unroll
    for (int k = 0; k < 3; ++k)
#pragma unroll
      for (int bp = 0; bp < 6; ++bp) x0[bp] = mfma16(lds128(smem, wob + (bp * 3 + k) * 1024), of[k], x0[bp]);
#pragma unroll
    for (int bp = 0; bp < 6; ++bp) {
      u32x2 o = {pk2(x0[bp][0], x0[bp][1]), pk2(x0[bp][2], x0[bp][3])};
      __builtin_amdgcn_raw_buffer_store_b64(o, srd, c.orel, obase + bp * 32, 0);
    }
  };

  auto window = [&](auto BUFC, int i) {
    constexpr int BUF = decltype(BUFC)::value;
    const int wi = wstart + i;
    const int wy = wi / NWS, wx = wi - wy * NWS;
    const unsigned obase = (unsigned)((wy * 7 * IMG + wx * 7) * PIXB);
    phase3(obase_prev, __builtin_amdgcn_make_buffer_rsrc(out, 0, i > 0 ? XBYTES : 0u, 0x00020000));
    obase_prev = obase;
    STAMP(1);
#pragma unroll
    for (int b = 0; b < 6; ++b) x0[b] = ZERO4;
#pragma unroll
    for (int s = 0; s < 5; ++s)
#pragma unroll
      for (int b = 0; b < 6; ++b)
        x0[b] = mfma16(lds128(smem, c.cta + (b * 5 + s) * 544),
                       lds128(smem, c.xa[s][b & 1] + BUF * 16384 + 64 * (b >> 1)), x0[b]);
    __builtin_amdgcn_sched_group_barrier(0x100, 16, 0);
#pragma unroll
    for (int k = 0; k < 18; ++k) {
      __builtin_amdgcn_sched_group_barrier(0x8, 1, 0);
      __builtin_amdgcn_sched_group_barrier(0x100, 1, 0);
    }
#pragma unroll
    for (int k = 0; k < 6; ++k) {
      __builtin_amdgcn_sched_group_barrier(0x8, 1, 0);
      __builtin_amdgcn_sched_group_barrier(0x2, 2, 0);
      __builtin_amdgcn_sched_group_barrier(0x40, 1, 0);
      __builtin_amdgcn_sched_group_barrier(0x100, 2, 0);
    }
#pragma unroll
    for (int k = 0; k < 24; ++k) {
      __builtin_amdgcn_sched_group_barrier(0x8, 1, 0);
      __builtin_amdgcn_sched_group_barrier(0x100, 2, 0);
    }
    STAMP(2);
    {
      u32x4 xb[3];
#pragma unroll
      for (int k = 0; k < 3; ++k)
        xb[k] = u32x4{pk2(x0[2 * k][0], x0[2 * k][1]), pk2(x0[2 * k][2], x0[2 * k][3]),
                      pk2(x0[2 * k + 1][0], x0[2 * k + 1][1]), pk2(x0[2 * k + 1][2], x0[2 * k + 1][3])};
      f32x4 s1 = ZERO4, s2 = ZERO4;
#pragma unroll
      for (int k = 0; k < 3; ++k) { s2 = mfma16(xb[k], xb[k], s2); s1 = mfma16(ONES, xb[k], s1); }
      float d = dm0 ? s2[0] : 0.0f;
      d = dm1 ? s2[1] : d;
      d = dm2 ? s2[2] : d;
      d = dm3 ? s2[3] : d;
      f32x4 sq = __builtin_amdgcn_mfma_f32_16x16x4f32(1.0f, d, ZERO4, 0, 0, 0);
      float mean = s1[0] * (1.0f / 96.0f);
      float var = fmaxf(fmaf(sq[0], 1.0f / 96.0f, -mean * mean), 0.0f);
      float rstd = __builtin_amdgcn_rsqf(var + 1e-5f);
      float nm = -mean * rstd;
#pragma unroll
      for (int k = 0; k < 3; ++k) {
        u32x4 hv;
#pragma unroll
        for (int u = 0; u < 2; ++u) {
          hv[2 * u] = pk2(fmaf(x0[2 * k + u][0], rstd, nm), fmaf(x0[2 * k + u][1], rstd, nm));
          hv[2 * u + 1] = pk2(fmaf(x0[2 * k + u][2], rstd, nm), fmaf(x0[2 * k + u][3], rstd, nm));
        }
        *(u32x4*)(smem + rw + k * 1024) = hv;
      }
    }
    STAMP(3);
    __builtin_amdgcn_sched_barrier(0);
    BAR_LGKM();
    STAMP(4);
    u32x4 hf[4][3];
#pragma unroll
    for (int T = 0; T < 4; ++T)
#pragma unroll
      for (int k = 0; k < 3; ++k) hf[T][k] = lds128(smem, RR + T * 3072 + k * 1024 + c.l16);
    unsigned vo[4];
    {
      const int wi2 = wi + 2;
      const int wy2 = wi2 / NWS, wx2 = wi2 - wy2 * NWS;
      const int inb = ((wy2 * 7 - 1) * IMG + (wx2 * 7 - 1)) * PIXB;
      const bool valid = i + 2 < nwin;
      const bool border = !valid || wy2 == 0 || wy2 == NWS - 1 || wx2 == 0 || wx2 == NWS - 1;
#pragma unroll
      for (int q = 0; q < 4; ++q) vo[q] = c.rel[q] + (unsigned)inb;
      if (border) {
#pragma unroll
        for (int q = 0; q < 4; ++q) {
          unsigned n = (w * 4 + q) * 64 + lane;
          unsigned pos = n / 12u;
          unsigned row = pos / 9u, col = pos - row * 9u;
          int gy = wy2 * 7 - 1 + (int)row, gx = wx2 * 7 - 1 + (int)col;
          bool ok = valid && n < 972u && gy >= 0 && gy < IMG && gx >= 0 && gx < IMG;
          vo[q] = ok ? vo[q] : OOB;
        }
      }
    }
    __builtin_amdgcn_sched_barrier(0);
    BAR_LGKM();
    __builtin_amdgcn_sched_barrier(0);
    STAMP(6);
    u32x4 Qf[4], Kf[4];
    u32x2 V0p[4], V1p[4];
#pragma unroll
    for (int T = 0; T < 4; ++T) {
      f32x4 q = ZERO4, kk = ZERO4, xx = ZERO4, v0 = ZERO4, v1 = cV1;
#pragma unroll
      for (int k = 0; k < 3; ++k) {
        q = mfma16(WQ[k], hf[T][k], q);
        kk = mfma16(WK[k], hf[T][k], kk);
        xx = mfma16(WX[k], hf[T][k], xx);
        v0 = mfma16(hf[T][k], WV0[k], v0);
        v1 = mfma16(hf[T][k], WV1[k], v1);
      }
      auto sw = __builtin_amdgcn_permlane32_swap(pk2(xx[0], xx[1]), pk2(xx[2], xx[3]), false, false);
      Qf[T] = u32x4{pk2(q[0], q[1]), pk2(q[2], q[3]), sw[0], 0u};
      Kf[T] = u32x4{pk2(kk[0], kk[1]), pk2(kk[2], kk[3]), sw[1], 0u};
      V0p[T] = u32x2{pk2(v0[0], v0[1]), pk2(v0[2], v0[3])};
      V1p[T] = u32x2{pk2(v1[0], v1[1]), pk2(v1[2], v1[3])};
    }
    const u32x4 VA0 = {V0p[0][0], V0p[0][1], V0p[1][0], V0p[1][1]};
    const u32x4 VA1 = {V0p[2][0], V0p[2][1], V0p[3][0], V0p[3][1]};
    const u32x4 VB0 = {V1p[0][0], V1p[0][1], V1p[1][0], V1p[1][1]};
    const u32x4 VB1 = {V1p[2][0], V1p[2][1], V1p[3][0], V1p[3][1]};
    STAMP(7);
    const unsigned dmadst = ldsbase + XB + BUF * 16384 + w * 4096;
    unsigned chk;
    {
      f32x4 d0 = mfma16(Qf[3], Kf[0], ZERO4);
      f32x4 d1 = mfma16(Qf[3], Kf[1], ZERO4);
      f32x4 d2 = mfma16(Qf[3], Kf[2], ZERO4);
      f32x4 d3 = mfma16(Qf[3], Kf[3], cS3);
      float v = d3[1];
      v = g2 ? d2[0] : v;
      v = g1 ? d1[0] : v;
      v = g0 ? d0[0] : v;
      *(unsigned short*)(smem + psw) = (unsigned short)pk2(__builtin_amdgcn_exp2f(v), 0.0f);
    }
    chk = 0;
#pragma unroll
    for (int T = 0; T < 3; ++T) {
      f32x4 s0 = mfma16(Kf[0], Qf[T], ZERO4);
      f32x4 s1 = mfma16(Kf[1], Qf[T], ZERO4);
      f32x4 s2 = mfma16(Kf[2], Qf[T], ZERO4);
      f32x4 s3 = mfma16(Kf[3], Qf[T], cS3);
      u32x4 p0, p1;
      p0[0] = pk2(__builtin_amdgcn_exp2f(s0[0]), __builtin_amdgcn_exp2f(s0[1]));
      p0[1] = pk2(__builtin_amdgcn_exp2f(s0[2]), __builtin_amdgcn_exp2f(s0[3]));
      p0[2] = pk2(__builtin_amdgcn_exp2f(s1[0]), __builtin_amdgcn_exp2f(s1[1]));
      p0[3] = pk2(__builtin_amdgcn_exp2f(s1[2]), __builtin_amdgcn_exp2f(s1[3]));
      p1[0] = pk2(__builtin_amdgcn_exp2f(s2[0]), __builtin_amdgcn_exp2f(s2[1]));
      p1[1] = pk2(__builtin_amdgcn_exp2f(s2[2]), __builtin_amdgcn_exp2f(s2[3]));
      p1[2] = pk2(__builtin_amdgcn_exp2f(s3[0]), 0.0f);
      p1[3] = 0u;
      f32x4 o0 = mfma16(VA0, p0, ZERO4);
      f32x4 o1 = mfma16(VB0, p0, ZERO4);
      o0 = mfma16(VA1, p1, o0);
      o1 = mfma16(VB1, p1, o1);
      const float lsum = o1[2];
      const float rl = __builtin_amdgcn_rcpf(lsum);
      chk = max(chk, max(fbits(lsum), fbits(rl)));
      char* op = smem + ow + T * 3072;
      *(unsigned*)(op) = pk2(o0[0] * rl, o0[1] * rl);
      *(unsigned*)(op + 1024) = pk2(o0[2] * rl, o0[3] * rl);
      *(unsigned*)(op + 2048) = pk2(o1[0] * rl, o1[1] * rl);
      dma16(dmadst + T * 1024, vo[T], xsrd, 0u);
    }
    {
      u32x4 p0 = lds128(smem, psr), p1 = lds128(smem, psr + 64);
      f32x4 o0 = mfma16(VA0, p0, ZERO4);
      f32x4 o1 = mfma16(VB0, p0, ZERO4);
      o0 = mfma16(VA1, p1, o0);
      o1 = mfma16(VB1, p1, o1);
      const float lsum = o1[2];
      const float rl = __builtin_amdgcn_rcpf(lsum);
      chk = max(chk, max(fbits(lsum), fbits(rl)));
      char* op = smem + ow + 3 * 3072;
      *(unsigned*)(op) = pk2(o0[0] * rl, o0[1] * rl);
      *(unsigned*)(op + 1024) = pk2(o0[2] * rl, o0[3] * rl);
      *(unsigned*)(op + 2048) = pk2(o1[0] * rl, o1[1] * rl);
      dma16(dmadst + 3 * 1024, vo[3], xsrd, 0u);
    }
    if (force_safe || __builtin_amdgcn_ballot_w64(chk >= 0x53800000u) != 0ull) {
#pragma unroll
      for (int T = 0; T < 4; ++T) {
        f32x4 s0 = mfma16(Kf[0], Qf[T], ZERO4);
        f32x4 s1 = mfma16(Kf[1], Qf[T], ZERO4);
        f32x4 s2 = mfma16(Kf[2], Qf[T], ZERO4);
        f32x4 s3 = mfma16(Kf[3], Qf[T], cS3);
        float m = fmaxf(fmaxf(fmaxf(s0[0], s0[1]), fmaxf(s0[2], s0[3])), fmaxf(fmaxf(s1[0], s1[1]), fmaxf(s1[2], s1[3])));
        m = fmaxf(m, fmaxf(fmaxf(fmaxf(s2[0], s2[1]), fmaxf(s2[2], s2[3])), s3[0]));
        m = fmaxf(m, __shfl_xor(m, 16));
        m = fmaxf(m, __shfl_xor(m, 32));
        u32x4 p0, p1;
        p0[0] = pk2(__builtin_amdgcn_exp2f(s0[0] - m), __builtin_amdgcn_exp2f(s0[1] - m));
        p0[1] = pk2(__builtin_amdgcn_exp2f(s0[2] - m), __builtin_amdgcn_exp2f(s0[3] - m));
        p0[2] = pk2(__builtin_amdgcn_exp2f(s1[0] - m), __builtin_amdgcn_exp2f(s1[1] - m));
        p0[3] = pk2(__builtin_amdgcn_exp2f(s1[2] - m), __builtin_amdgcn_exp2f(s1[3] - m));
        p1[0] = pk2(__builtin_amdgcn_exp2f(s2[0] - m), __builtin_amdgcn_exp2f(s2[1] - m));
        p1[1] = pk2(__builtin_amdgcn_exp2f(s2[2] - m), __builtin_amdgcn_exp2f(s2[3] - m));
        p1[2] = pk2(__builtin_amdgcn_exp2f(s3[0] - m), 0.0f);
        p1[3] = 0u;
        f32x4 o0 = mfma16(VA0, p0, ZERO4);
        f32x4 o1 = mfma16(VB0, p0, ZERO4);
        o0 = mfma16(VA1, p1, o0);
        o1 = mfma16(VB1, p1, o1);
        float rl = __builtin_amdgcn_rcpf(o1[2]);
        char* op = smem + ow + T * 3072;
        *(unsigned*)(op) = pk2(o0[0] * rl, o0[1] * rl);
        *(unsigned*)(op + 1024) = pk2(o0[2] * rl, o0[3] * rl);
        *(unsigned*)(op + 2048) = pk2(o1[0] * rl, o1[1] * rl);
      }
    }
    STAMP(8);
    BAR_VM4();
    STAMP(9);
  };

  struct B0 { enum { value = 0 }; };
  struct B1 { enum { value = 1 }; };
  for (int i = 0; i < nwin; i += 2) {
    window(B0{}, i);
    if (i + 1 >= nwin) break;
    window(B1{}, i + 1);
  }
  phase3(obase_prev, osrd);
  STAMP(10);
}

extern "C" void kernel_launch(void* const* d_in, const int* in_sizes, int n_in,
                              void* d_out, int out_size, void* d_ws, size_t ws_size,
                              hipStream_t stream) {
  const unsigned short* x = (const unsigned short*)d_in[0];
  const unsigned short* dw = (const unsigned short*)d_in[1];
  const unsigned short* wqkv = (const unsigned short*)d_in[2];
  const unsigned short* wo = (const unsigned short*)d_in[3];
  davit_win_attn_kernel<<<NBLK, 256, 0, stream>>>(x, dw, wqkv, wo, (unsigned short*)d_out
  );
}
```

```cpp
#include <hip/hip_runtime.h>
#include <hip/hip_bf16.h>
#include <stdint.h>

typedef __attribute__((ext_vector_type(8))) __bf16 bf16x8;
typedef __attribute__((ext_vector_type(16))) __bf16 bf16x16;
typedef __attribute__((ext_vector_type(2))) __bf16 bf16x2;
typedef __attribute__((ext_vector_type(4))) float f32x4;
typedef __attribute__((ext_vector_type(2))) float f32x2;
typedef __attribute__((ext_vector_type(4))) unsigned u32x4;
typedef __attribute__((ext_vector_type(8))) unsigned u32x8;
typedef __attribute__((ext_vector_type(2))) unsigned u32x2;
typedef __attribute__((ext_vector_type(4))) short s16x4;
typedef __attribute__((ext_vector_type(2))) unsigned short u16x2;
typedef __attribute__((address_space(3))) s16x4* lds_s16x4_p;

#define A_PRIO 2
#define B_PRIO 1
#define C_PRIO 2
#define B_DELAY 6
#define PRO_A 2
#define PRO_B 1
#define P2_A 2
#define P2_B 1
#define ST_AUX 16
#define LD_POL ""
#define DEVI __device__ __forceinline__
#define SB __builtin_amdgcn_sched_barrier(0)

constexpr int IMG = 511;
constexpr int NWS = 73;
constexpr int NWIN = NWS * NWS;
constexpr int PIXB = 192;
constexpr unsigned XBYTES = (unsigned)IMG * IMG * PIXB;
constexpr unsigned OOB = 0x80000000u;

constexpr int XB = 0;
constexpr int H1 = 65536;
constexpr int XQ = H1 + 3 * 9216;
constexpr int HL1 = XQ + 6144;
constexpr int OL = HL1 + 3072;
constexpr int H0 = OL + 6144;
constexpr int OR_ = H0 + 9216;
constexpr int HL0 = OR_ + 18432;
constexpr int WO = HL0 + 3072;
constexpr int XL = WO + 12288;
constexpr int XLS = 2112;
constexpr int PS = XL + 4 * XLS;
constexpr int LDS_BYTES = PS + 512;
constexpr int RAWQ = 49152;
constexpr int RAWO = OR_;
static_assert(RAWQ + 4 * 14336 <= H0, "W_qkv staging overlaps live data");
constexpr int hslot(int s) { return s == 0 ? H0 - H1 : (s - 1) * 9216; }

#define PF2_STEP 16
#define TPA 0
#define TPB 2
#define TPC 0
#define CODE_BYTES 0x10800
constexpr int NBLK = 256;
constexpr int PERX = NBLK / 8;
constexpr int NTHR = 512;

DEVI f32x4 mfma16(u32x4 a, u32x4 b, f32x4 c) {
  return __builtin_amdgcn_mfma_f32_16x16x32_bf16(__builtin_bit_cast(bf16x8, a), __builtin_bit_cast(bf16x8, b), c, 0, 0, 0);
}
DEVI f32x4 smf(u32x4 a, u32x4 b0, u32x4 b1, f32x4 c, unsigned idx) {
  const u32x8 b = __builtin_shufflevector(b0, b1, 0, 1, 2, 3, 4, 5, 6, 7);
  return __builtin_amdgcn_smfmac_f32_16x16x64_bf16(__builtin_bit_cast(bf16x8, a), __builtin_bit_cast(bf16x16, b), c, (int)idx, 0, 0);
}
DEVI unsigned pk2(float a, float b) {
  bf16x2 v = {(__bf16)a, (__bf16)b};
  return __builtin_bit_cast(unsigned, v);
}
DEVI unsigned szero() {
  unsigned z;
  asm volatile("s_mov_b32 %0, 0" : "=s"(z));
  return z;
}
DEVI unsigned fbits(float f) { return __builtin_bit_cast(unsigned, f); }
DEVI float bf_lo(unsigned u) { return __builtin_bit_cast(float, u << 16); }
DEVI float bf_hi(unsigned u) { return __builtin_bit_cast(float, u & 0xffff0000u); }

template <int IMM>
DEVI void dma16(unsigned lds_base, unsigned voff, u32x4 srd, unsigned soff) {
  asm volatile("s_add_i32 m0, %0, %4\n\ts_nop 0\n\tbuffer_load_dwordx4 %1, %2, %3 offen lds"
               :: "s"(lds_base), "v"(voff), "s"(srd), "s"(soff), "n"(IMM) : "memory", "scc");
}
template <int IMM>
DEVI void dma16x(unsigned lds_base, unsigned voff, u32x4 srd, unsigned soff) {
  asm volatile("s_add_i32 m0, %0, %4\n\ts_nop 0\n\tbuffer_load_dwordx4 %1, %2, %3 offen" LD_POL " lds"
               :: "s"(lds_base), "v"(voff), "s"(srd), "s"(soff), "n"(IMM) : "memory", "scc");
}
DEVI unsigned lane_id_opaque() {
  unsigned l;
  asm volatile("v_mbcnt_lo_u32_b32 %0, -1, 0\n\tv_mbcnt_hi_u32_b32 %0, -1, %0" : "=v"(l));
  return l;
}
#define BAR_LGKM() asm volatile("s_waitcnt lgkmcnt(0)\n\ts_barrier" ::: "memory")
#define BAR_VM(N)  asm volatile("s_waitcnt vmcnt(" #N ") lgkmcnt(0)\n\ts_barrier" ::: "memory")
template <int N> DEVI void bar_vm() { asm volatile("s_waitcnt vmcnt(%0) lgkmcnt(0)\n\ts_barrier" :: "n"(N) : "memory"); }
constexpr int DDM = 0
;
constexpr int CBAR[4] = { 24, 18, 21, 29
};
constexpr int EARLY1 = 0
;

DEVI u32x4 lds128(const char* smem, int off) { return *(const u32x4*)(smem + off); }

DEVI u32x2 trread(const char* smem, int off) {
  s16x4 v = __builtin_amdgcn_ds_read_tr16_b64_v4i16((lds_s16x4_p)(smem + off));
  return __builtin_bit_cast(u32x2, v);
}

#define STAMP(k) do {} while (0)

#define TS(i) do {} while (0)
#define TSOUT() do {} while (0)
#define TSL(i) do {} while (0)
#define TSLOUT() do {} while (0)
template <int V> struct IC { enum { value = V }; };

DEVI void wo_frags(const char* smem, int bp, int g, int t, u32x4& f0, u32x4& f1, u32x4& f2) {
  const int base = RAWO + (bp >> 1) * 6144 + (24 * g + (t >> 2)) * 64 + (8 * (t & 3) + 4 * (bp & 1)) * 2;
#pragma unroll
  for (int gs = 0; gs < 4; ++gs) {
    u32x2 a = trread(smem, base + 4 * gs * 64);
    f0[gs] = a[0]; f1[gs] = a[1];
  }
#pragma unroll
  for (int u = 0; u < 2; ++u) {
    u32x2 a = trread(smem, base + (16 + 4 * u) * 64);
    f2[2 * u] = a[0]; f2[2 * u + 1] = a[1];
  }
}
DEVI void wo_stage(unsigned ldsbase, const unsigned short* wo, int k, int lane) {
  u32x4 osr;
  osr[0] = (unsigned)(size_t)wo; osr[1] = (unsigned)((size_t)wo >> 32) & 0xffffu; osr[2] = 18432u; osr[3] = 0x00020000u;
  const unsigned vo = (lane >> 2) * 192 + (lane & 3) * 16 + k * 64;
  const unsigned dst = ldsbase + k * 6144;
  dma16<RAWO>(dst, vo, osr, 0u); dma16<RAWO + 1024>(dst, vo, osr, 3072u); dma16<RAWO + 2048>(dst, vo, osr, 6144u);
  dma16<RAWO + 3072>(dst, vo, osr, 9216u); dma16<RAWO + 4096>(dst, vo, osr, 12288u); dma16<RAWO + 5120>(dst, vo, osr, 15360u);
}
DEVI unsigned tile_src(unsigned n, bool piece13) { return piece13 ? (n - 864u < 12u ? n + 96u : n) : n; }
DEVI unsigned tile_rel(unsigned n, int dl) {
  const unsigned row = __umul24(n, 607u) >> 16;
  const int sw = (int)((__umul24(n, 683u) >> 15) & 1u);
  return n * 16u + __umul24(row, (unsigned)(IMG * PIXB - 1728)) + (unsigned)__mul24(sw, dl);
}
DEVI unsigned tile_cut(unsigned n, bool left, bool right) {
  const unsigned c = n - __umul24(__umul24(n, 607u) >> 16, 108u);
  return ((left & (c < 12u)) | (right & (c >= 96u))) ? 0xc0000000u : 0u;
}
#define WAIT_VM(N) asm volatile("s_waitcnt vmcnt(" #N ")" ::: "memory")

__global__ __launch_bounds__(512, 2)
void davit_win_attn_kernel(const unsigned short* x, const unsigned short* dw,
                           const unsigned short* wqkv, const unsigned short* wo,
                           unsigned short* out
                           ) {
  __shared__ __attribute__((aligned(16))) char smem[LDS_BYTES];
  const int tid = threadIdx.x;
  const int lane = tid & 63;
  const int w8 = __builtin_amdgcn_readfirstlane(tid >> 6);
  const int w = w8 & 3;
  const int g = lane >> 4, t = lane & 15;
  const unsigned ldsbase = (unsigned)(size_t)smem;
  const unsigned l16 = lane * 16;

  const int bid = blockIdx.x;
  const int xcd = bid & 7, idx = bid >> 3;
  const int xs = (NWIN * xcd) >> 3, xe = (NWIN * (xcd + 1)) >> 3;
  const int cnt = xe - xs;
  const int wstart = xs + idx;
  const int nwin = (cnt - idx + PERX - 1) / PERX;

  const u32x4 ONES = {0x3f803f80u, 0x3f803f80u, 0x3f803f80u, 0x3f803f80u};
  const f32x4 ZERO4 = {0.f, 0.f, 0.f, 0.f};

  STAMP(16);
  unsigned long long pc;
  asm volatile("s_getpc_b64 %0" : "=s"(pc));
  {
    u32x4 csrd;
    csrd[0] = (unsigned)pc & ~255u; csrd[1] = (unsigned)(pc >> 32) & 0xffffu; csrd[2] = CODE_BYTES; csrd[3] = 0x00020000u;
    const unsigned c = (unsigned)(idx * 8 + w8) * 1024u;
    dma16<WO>(ldsbase, l16 + c, csrd, 0u);
  }
  STAMP(12);
  u32x4 xsrd;
  auto args_ready = [&]() {
    asm volatile("" : "+s"(x), "+s"(dw), "+s"(wqkv), "+s"(wo), "+s"(out));
    xsrd[0] = (unsigned)(size_t)x; xsrd[1] = (unsigned)((size_t)x >> 32) & 0xffffu; xsrd[2] = XBYTES; xsrd[3] = 0x00020000u;
  };
  auto win_base = [&](int m, int& cy, int& cx) -> unsigned {
    const int wi = wstart + m * PERX;
    cy = wi / NWS; cx = wi - cy * NWS;
    return (unsigned)((cy * 7 * IMG + cx * 7) * PIXB);
  };
  struct Cur { int cy, cx; unsigned base; };
  auto cur_at = [&](int m) { Cur c; c.base = win_base(m, c.cy, c.cx); return c; };
  auto xfree_of = [](int nw) { return (unsigned)(H1 + hslot(1) + (nw & 1) * 9216); };
  constexpr unsigned STEP_IN = PERX * 7 * PIXB, STEP_WRAP = (7 * IMG + (PERX - NWS) * 7) * PIXB;
  auto cur_next = [&](Cur& c) {
    const bool wrap = c.cx >= NWS - PERX;
    c.base += wrap ? STEP_WRAP : STEP_IN;
    c.cy += wrap ? 1 : 0;
    c.cx += wrap ? PERX - NWS : PERX;
  };
  unsigned rel4[4];
  auto make_rel1 = [&](int q) {
    const unsigned ln = lane_id_opaque() & 63u;
    const int dl = (ln & 2) ? -32 : 32;
    const unsigned n = (w * 4 + q) * 64 + ln;
    unsigned r = tile_rel(tile_src(n, q == 1), dl);
    if (q == 3) { asm volatile("" : "+v"(r)); r = n < 960u ? r : OOB; }
    rel4[q] = r;
    asm volatile("" : "+v"(rel4[q]));
  };
  auto make_rel4 = [&]() {
    const unsigned ln = lane_id_opaque() & 63u;
    const int dl = (ln & 2) ? -32 : 32;
#pragma unroll
    for (int q = 0; q < 4; ++q) {
      const unsigned n = (w * 4 + q) * 64 + ln;
      unsigned r = tile_rel(tile_src(n, q == 1), dl);
      if (q == 3) { asm volatile("" : "+v"(r)); r = n < 960u ? r : OOB; }
      rel4[q] = r;
    }
    asm volatile("" : "+v"(rel4[0]), "+v"(rel4[1]), "+v"(rel4[2]), "+v"(rel4[3]));
  };
  auto tile4 = [&](auto MC, const Cur& pc) {
    constexpr int m = decltype(MC)::value;
    const unsigned dst = ldsbase + XB + w * 4096;
    const unsigned inb = pc.base - (unsigned)((IMG + 1) * PIXB);
    unsigned vo[4];
#pragma unroll
    for (int q = 0; q < 4; ++q) vo[q] = rel4[q] + inb;
    if (__builtin_expect((pc.cx == 0) | (pc.cx == NWS - 1), 0)) {
      const unsigned ln = lane_id_opaque() & 63u;
#pragma unroll
      for (int q = 0; q < 4; ++q) vo[q] |= tile_cut(tile_src((w * 4 + q) * 64 + ln, q == 1), pc.cx == 0, pc.cx == NWS - 1);
    }
    dma16x<m * 16384 + 0>(dst, vo[0], xsrd, 0u);
    dma16x<m * 16384 + 1024>(dst, vo[1], xsrd, 0u);
    dma16x<m * 16384 + 2048>(dst, vo[2], xsrd, 0u);
    dma16x<m * 16384 + 3072>(dst, vo[3], xsrd, 0u);
  };

  const int dwst = w < 3 ? H0 + w * 3072 : HL0;
  STAMP(14);
  unsigned relL[2];
  auto xl_one = [&](unsigned dst, const Cur& xc, bool on) {
    u32x4 srd = xsrd;
    srd[2] = on ? XBYTES : 0u;
    unsigned v0 = relL[0], v1 = relL[1];
    if (__builtin_expect((xc.cx == 0) | (xc.cy == NWS - 1), 0)) {
      const unsigned ln = lane_id_opaque() & 63u;
      const unsigned px0 = __umul24(ln, 43u) >> 9, px1 = __umul24(64u + ln, 43u) >> 9;
      const unsigned row0 = __umul24(px0, 11u) >> 5, row1 = __umul24(px1, 11u) >> 5;
      const bool left = xc.cx == 0, low = xc.cy == NWS - 1;
      v0 |= ((left & (px0 == 3u * row0)) | (low & (row0 == 2u))) ? 0xc0000000u : 0u;
      v1 |= ((left & (px1 == 3u * row1)) | (low & (row1 == 2u))) ? 0xc0000000u : 0u;
    }
    dma16x<XL>(dst, v0, srd, xc.base);
    dma16x<XL + 1024>(dst, v1, srd, xc.base);
  };
  unsigned wvo;
  if (w8 >= 4) {
    __builtin_amdgcn_s_setprio(3);
    make_rel4();
    Cur cw = cur_at(w);
    Cur c0 = cur_at(0);
    asm volatile("" : "+s"(c0.base), "+s"(c0.cx), "+s"(cw.base), "+s"(cw.cx), "+s"(cw.cy));
    SB;
    STAMP(17);
    args_ready();
    TS(1);
    STAMP(18);
    u32x4 dsrd;
    dsrd[0] = (unsigned)(size_t)dw; dsrd[1] = (unsigned)((size_t)dw >> 32) & 0xffffu; dsrd[2] = 1728u; dsrd[3] = 0x00020000u;
    dma16<0>(ldsbase + dwst, l16, dsrd, 0u);
    dma16<1024>(ldsbase + dwst, l16 + 1024u, dsrd, 0u);
    tile4(IC<0>{}, c0);
    SB;
#pragma unroll
    for (int q = 0; q < 2; ++q) {
      const unsigned n = q * 64 + lane;
      const unsigned px = __umul24(n, 43u) >> 9, cc = n - px * 12u;
      const unsigned row = __umul24(px, 11u) >> 5, col = px - row * 3u;
      unsigned r = __umul24(5 + row, (unsigned)(IMG * PIXB)) + col * PIXB + cc * 16u - PIXB;
      if (q == 1) { asm volatile("" : "+v"(r)); r = n < 108u ? r : OOB; }
      relL[q] = r;
    }
    asm volatile("" : "+v"(relL[0]), "+v"(relL[1]));
    xl_one(ldsbase + w * XLS, cw, true);
  } else {
    __builtin_amdgcn_s_setprio(PRO_B);
    const unsigned r7 = __umul24(lane, 57u) >> 9, rc = lane - r7 * 9u, seg = __umul24(rc, 11u) >> 5;
    wvo = r7 * 576u + seg * 192u + (rc - seg * 3u) * 16u + w * 48u;
    asm volatile("" : "+v"(wvo));
    wvo = lane < 63 ? wvo : OOB;
    asm volatile("" : "+v"(wvo));
    SB;
    STAMP(7);
    args_ready();
    TS(1);
    const int c = idx * 4 + w;
    const unsigned short* wp = c < 54 ? wqkv : wo;
    u32x4 psrd;
    psrd[0] = (unsigned)(size_t)wp; psrd[1] = (unsigned)((size_t)wp >> 32) & 0xffffu; psrd[2] = c < 54 ? 55296u : 18432u; psrd[3] = 0x00020000u;
    dma16<WO>(ldsbase, l16 + (c < 54 ? c : c - 54) * 1024u, psrd, 0u);
  }
  asm volatile("s_barrier" ::: "memory");
  int role = w8;
  asm volatile("" : "+s"(role));
  if (role >= 4) {
    STAMP(15);
    TS(2);
    __builtin_amdgcn_s_setprio(PRO_A);
    auto first_tile_landed = [&]() {
      BAR_VM(0);
    };
    const __amdgpu_buffer_rsrc_t osrd = __builtin_amdgcn_make_buffer_rsrc(out, 0, XBYTES, 0x00020000);
    STAMP(7);
    u32x4 CD[6], CS[12];
    auto build_conv = [&]() {
      const int hf = g >> 1, e = g & 1;
      const int tapA = g == 0 ? 1 : (g == 1 ? 4 : (g == 2 ? 2 : 5));
      const int tapB = g == 0 ? 4 : (g == 1 ? 7 : (g == 2 ? 6 : 8));
      const unsigned short* dws = (const unsigned short*)(smem + dwst);
      unsigned short dv[18];
#pragma unroll
      for (int b = 0; b < 6; ++b) {
        dv[b] = dws[hf * 3 * 96 + b * 16 + t];
        dv[6 + 2 * b] = dws[tapA * 96 + b * 16 + t];
        dv[7 + 2 * b] = dws[tapB * 96 + b * 16 + t];
      }
      const bool act = e == (t >> 3);
      const int d = (t & 7) >> 1;
      unsigned m[4], ms[4];
#pragma unroll
      for (int j = 0; j < 4; ++j) { m[j] = (act & (d == j)) ? 0xffffffffu : 0u; ms[j] = (t >> 2) == j ? 0xffffu : 0u; }
      const int sh = (t & 1) * 16;
#pragma unroll
      for (int b = 0; b < 6; ++b) {
        const unsigned v = (unsigned)dv[b] << sh;
        CD[b] = u32x4{v & m[0], v & m[1], v & m[2], v & m[3]};
        const unsigned va = dv[6 + 2 * b];
        const unsigned vb = g == 0 ? 0x3f80u : (unsigned)dv[7 + 2 * b];
        CS[2 * b] = u32x4{va & ms[0], va & ms[1], va & ms[2], va & ms[3]};
        CS[2 * b + 1] = u32x4{vb & ms[0], vb & ms[1], vb & ms[2], vb & ms[3]};
        asm volatile("" : "+v"(CD[b]), "+v"(CS[2 * b]), "+v"(CS[2 * b + 1]));
      }
    };
    const unsigned sidx = (unsigned)((t & 3) | (((t + 1) & 3) << 2)) * 0x1111u;
    u32x4 SEL0, SEL1;
    {
      const unsigned one = 0x3f80u << ((t & 1) * 16);
      const int d = 2 * (t >> 3) + ((t & 3) >> 1);
      const bool on0 = g == 2 * ((t >> 2) & 1), on1 = g == 2 * ((t >> 2) & 1) + 1;
#pragma unroll
      for (int j = 0; j < 4; ++j) { SEL0[j] = (on0 && d == j) ? one : 0u; SEL1[j] = (on1 && d == j) ? one : 0u; }
    }
    const bool dm0 = lane == 0 + 0 || lane == 16 + 4 || lane == 32 + 8 || lane == 48 + 12;
    const bool dm1 = lane == 0 + 1 || lane == 16 + 5 || lane == 32 + 9 || lane == 48 + 13;
    const bool dm2 = lane == 0 + 2 || lane == 16 + 6 || lane == 32 + 10 || lane == 48 + 14;
    const bool dm3 = lane == 0 + 3 || lane == 16 + 7 || lane == 32 + 11 || lane == 48 + 15;
    auto tok = [&](int T, int& ty, int& tx) {
      int set = 2 * T + ((t >= 4 && t < 12) ? 1 : 0);
      int r = t < 4 ? t : (t < 12 ? t - 4 : t - 8);
      ty = set + (set >= ((r + 1) & 7) ? 1 : 0);
      tx = (r - ty) & 7;
    };

    if (w < 3) {
      unsigned xa[5][2], orel;
      {
        int ty, tx;
        tok(w, ty, tx);
        const int pos = 9 * (ty + 1) + (tx + 1);
        const int hf = g >> 1, e = g & 1;
        const int d0[5] = {-10, -9, -8, 0, 8};
        const int d1[5] = {-1, 0, 1, 9, 10};
#pragma unroll
        for (int s = 0; s < 5; ++s) {
          int p = pos + (hf ? d1[s] : d0[s]);
          if (s == 4) p = p == 80 ? 72 : p;
          unsigned a = XB + p * 192 + e * 16;
          unsigned sw = ((p >> 2) & 1) * 32;
          xa[s][0] = a + sw;
          xa[s][1] = a + (sw ^ 32u);
        }
      }
      unsigned rw = H1 + w * 3072 + l16;
      unsigned rq = XQ + w * 1024 + l16;
      unsigned wob = WO + l16;
      asm volatile("" : "+v"(rw), "+v"(rq), "+v"(wob));
      u32x4 XS0[3], XS1[3], XS2[3], XS3[3];
      asm volatile("" : "+v"(xa[0][0]), "+v"(xa[0][1]), "+v"(xa[1][0]), "+v"(xa[1][1]), "+v"(xa[2][0]), "+v"(xa[2][1]), "+v"(xa[3][0]), "+v"(xa[3][1]),
                        "+v"(xa[4][0]), "+v"(xa[4][1]));
      *(u32x4*)(smem + WO + 1024 + w * 1024 + l16) = u32x4{rel4[0], rel4[1], rel4[2], rel4[3]};
      WAIT_VM(6);
      TS(3);
      STAMP(8);
      build_conv();
      STAMP(9);
      TS(4);
      first_tile_landed();
      TS(5);
      STAMP(0);
      {
        constexpr int XOFF = 0, HOFF = hslot(0);
        u32x4* XN = XS0;
        const u32x4 cb0_0 = lds128(smem, xa[0][0] + XOFF + 0); SB;
        const u32x4 cb1_0 = lds128(smem, xa[0][1] + XOFF + 0); SB;
        const u32x4 cb0_1 = lds128(smem, xa[1][0] + XOFF + 0); SB;
        const u32x4 cb1_1 = lds128(smem, xa[1][1] + XOFF + 0); SB;
        const u32x4 cb0_2 = lds128(smem, xa[2][0] + XOFF + 0); SB;
        const u32x4 cb1_2 = lds128(smem, xa[2][1] + XOFF + 0); SB;
        const u32x4 cb0_3 = lds128(smem, xa[3][0] + XOFF + 0); SB;
        const u32x4 cb1_3 = lds128(smem, xa[3][1] + XOFF + 0); SB;
        const f32x4 xn0_0 = mfma16(CD[0], cb0_0, ZERO4); SB;
        const u32x4 cb0_4 = lds128(smem, xa[4][0] + XOFF + 0); SB;
        const f32x4 xn1_0 = mfma16(CD[1], cb1_0, ZERO4); SB;
        const u32x4 cb1_4 = lds128(smem, xa[4][1] + XOFF + 0); SB;
        const u32x4 cb2_0 = lds128(smem, xa[0][0] + XOFF + 64); SB;
        const u32x4 cb3_0 = lds128(smem, xa[0][1] + XOFF + 64); SB;
        const f32x4 xn0_1 = smf(CS[0], cb0_1, cb0_2, xn0_0, sidx); SB;
        const u32x4 cb2_1 = lds128(smem, xa[1][0] + XOFF + 64); SB;
        const f32x4 xn1_1 = smf(CS[2], cb1_1, cb1_2, xn1_0, sidx); SB;
        const u32x4 cb3_1 = lds128(smem, xa[1][1] + XOFF + 64); SB;
        const u32x4 cb2_2 = lds128(smem, xa[2][0] + XOFF + 64); SB;
        const u32x4 cb3_2 = lds128(smem, xa[2][1] + XOFF + 64); SB;
        const f32x4 xn0_2 = smf(CS[1], cb0_3, cb0_4, xn0_1, sidx); SB;
        const u32x4 cb2_3 = lds128(smem, xa[3][0] + XOFF + 64); SB;
        const f32x4 xn1_2 = smf(CS[3], cb1_3, cb1_4, xn1_1, sidx); SB;
        const u32x4 cb3_3 = lds128(smem, xa[3][1] + XOFF + 64); SB;
        const f32x4 xn2_0 = mfma16(CD[2], cb2_0, ZERO4); SB;
        const u32x4 cb2_4 = lds128(smem, xa[4][0] + XOFF + 64); SB;
        const f32x4 xn3_0 = mfma16(CD[3], cb3_0, ZERO4); SB;
        const u32x4 cb3_4 = lds128(smem, xa[4][1] + XOFF + 64); SB;
        const unsigned xb0_0 = pk2(xn0_2[0], xn0_2[1]); SB;
        const unsigned xb0_1 = pk2(xn0_2[2], xn0_2[3]); SB;
        const unsigned xb0_2 = pk2(xn1_2[0], xn1_2[1]); SB;
        const unsigned xb0_3 = pk2(xn1_2[2], xn1_2[3]); SB;
        const u32x4 xt0 = u32x4{xb0_0, xb0_1, xb0_2, xb0_3}; XN[0] = xt0;
        const f32x4 s2_0 = mfma16(xt0, xt0, ZERO4); SB;
        const u32x4 cb4_0 = lds128(smem, xa[0][0] + XOFF + 128); SB;
        const u32x4 cb5_0 = lds128(smem, xa[0][1] + XOFF + 128); SB;
        const f32x4 s1_0 = mfma16(ONES, xt0, ZERO4); SB;
        const u32x4 cb4_1 = lds128(smem, xa[1][0] + XOFF + 128); SB;
        const u32x4 cb5_1 = lds128(smem, xa[1][1] + XOFF + 128); SB;
        const f32x4 xn2_1 = smf(CS[4], cb2_1, cb2_2, xn2_0, sidx); SB;
        const u32x4 cb4_2 = lds128(smem, xa[2][0] + XOFF + 128); SB;
        const f32x4 xn3_1 = smf(CS[6], cb3_1, cb3_2, xn3_0, sidx); SB;
        const u32x4 cb5_2 = lds128(smem, xa[2][1] + XOFF + 128); SB;
        const f32x4 xn2_2 = smf(CS[5], cb2_3, cb2_4, xn2_1, sidx); SB;
        const u32x4 cb4_3 = lds128(smem, xa[3][0] + XOFF + 128); SB;
        const f32x4 xn3_2 = smf(CS[7], cb3_3, cb3_4, xn3_1, sidx); SB;
        const u32x4 cb5_3 = lds128(smem, xa[3][1] + XOFF + 128); SB;
        const f32x4 xn4_0 = mfma16(CD[4], cb4_0, ZERO4); SB;
        const u32x4 cb4_4 = lds128(smem, xa[4][0] + XOFF + 128); SB;
        const f32x4 xn5_0 = mfma16(CD[5], cb5_0, ZERO4); SB;
        const u32x4 cb5_4 = lds128(smem, xa[4][1] + XOFF + 128); SB;
        const unsigned xb1_0 = pk2(xn2_2[0], xn2_2[1]); SB;
        const unsigned xb1_1 = pk2(xn2_2[2], xn2_2[3]); SB;
        const unsigned xb1_2 = pk2(xn3_2[0], xn3_2[1]); SB;
        const unsigned xb1_3 = pk2(xn3_2[2], xn3_2[3]); SB;
        const u32x4 xt1 = u32x4{xb1_0, xb1_1, xb1_2, xb1_3}; XN[1] = xt1;
        const f32x4 s2_1 = mfma16(xt1, xt1, s2_0); SB;
        const f32x4 xn4_1 = smf(CS[8], cb4_1, cb4_2, xn4_0, sidx); SB;
        const f32x4 xn5_1 = smf(CS[10], cb5_1, cb5_2, xn5_0, sidx); SB;
        const f32x4 s1_1 = mfma16(ONES, xt1, s1_0); SB;
        const f32x4 xn4_2 = smf(CS[9], cb4_3, cb4_4, xn4_1, sidx); SB;
        const f32x4 xn5_2 = smf(CS[11], cb5_3, cb5_4, xn5_1, sidx); SB;
        const unsigned xb2_0 = pk2(xn4_2[0], xn4_2[1]); SB;
        const unsigned xb2_1 = pk2(xn4_2[2], xn4_2[3]); SB;
        const unsigned xb2_2 = pk2(xn5_2[0], xn5_2[1]); SB;
        const unsigned xb2_3 = pk2(xn5_2[2], xn5_2[3]); SB;
        const u32x4 xt2 = u32x4{xb2_0, xb2_1, xb2_2, xb2_3}; XN[2] = xt2;
        const f32x4 s2_2 = mfma16(xt2, xt2, s2_1); SB;
        const f32x4 s1_2 = mfma16(ONES, xt2, s1_1); SB;
        const float d0 = dm0 ? s2_2[0] : 0.0f; SB;
        const float d1 = dm1 ? s2_2[1] : d0; SB;
        const float d2 = dm2 ? s2_2[2] : d1; SB;
        const float d3 = dm3 ? s2_2[3] : d2; SB;
        const float mean = s1_2[0] * (1.0f / 96.0f); SB;
        const float tv = fmaf(-mean, mean, 1e-5f); SB;
        const f32x4 sq = __builtin_amdgcn_mfma_f32_16x16x4f32(1.0f, d3, ZERO4, 0, 0, 0); SB;
        const float var = fmaf(sq[0], 1.0f / 96.0f, tv); SB;
        const float varc = __builtin_amdgcn_fmed3f(var, 1e-5f, 3.0e38f); SB;
        const float rstd = __builtin_amdgcn_rsqf(varc); SB;
        const float nm = -mean * rstd; SB;
        const float h0_0_0 = fmaf(xn0_2[0], rstd, nm); SB;
        const float h0_0_1 = fmaf(xn0_2[1], rstd, nm); SB;
        const float h0_1_0 = fmaf(xn0_2[2], rstd, nm); SB;
        const float h0_1_1 = fmaf(xn0_2[3], rstd, nm); SB;
        const float h0_2_0 = fmaf(xn1_2[0], rstd, nm); SB;
        const float h0_2_1 = fmaf(xn1_2[1], rstd, nm); SB;
        const float h0_3_0 = fmaf(xn1_2[2], rstd, nm); SB;
        const float h0_3_1 = fmaf(xn1_2[3], rstd, nm); SB;
        const float h1_0_0 = fmaf(xn2_2[0], rstd, nm); SB;
        const float h1_0_1 = fmaf(xn2_2[1], rstd, nm); SB;
        const float h1_1_0 = fmaf(xn2_2[2], rstd, nm); SB;
        const float h1_1_1 = fmaf(xn2_2[3], rstd, nm); SB;
        const float h1_2_0 = fmaf(xn3_2[0], rstd, nm); SB;
        const float h1_2_1 = fmaf(xn3_2[1], rstd, nm); SB;
        const float h1_3_0 = fmaf(xn3_2[2], rstd, nm); SB;
        const float h1_3_1 = fmaf(xn3_2[3], rstd, nm); SB;
        const float h2_0_0 = fmaf(xn4_2[0], rstd, nm); SB;
        const float h2_0_1 = fmaf(xn4_2[1], rstd, nm); SB;
        const float h2_1_0 = fmaf(xn4_2[2], rstd, nm); SB;
        const float h2_1_1 = fmaf(xn4_2[3], rstd, nm); SB;
        const float h2_2_0 = fmaf(xn5_2[0], rstd, nm); SB;
        const float h2_2_1 = fmaf(xn5_2[1], rstd, nm); SB;
        const float h2_3_0 = fmaf(xn5_2[2], rstd, nm); SB;
        const float h2_3_1 = fmaf(xn5_2[3], rstd, nm); SB;
        const unsigned hv0_0 = pk2(h0_0_0, h0_0_1); SB;
        const unsigned hv0_1 = pk2(h0_1_0, h0_1_1); SB;
        const unsigned hv0_2 = pk2(h0_2_0, h0_2_1); SB;
        const unsigned hv0_3 = pk2(h0_3_0, h0_3_1); SB;
        const unsigned hv1_0 = pk2(h1_0_0, h1_0_1); SB;
        const unsigned hv1_1 = pk2(h1_1_0, h1_1_1); SB;
        const unsigned hv1_2 = pk2(h1_2_0, h1_2_1); SB;
        const unsigned hv1_3 = pk2(h1_3_0, h1_3_1); SB;
        const unsigned hv2_0 = pk2(h2_0_0, h2_0_1); SB;
        const unsigned hv2_1 = pk2(h2_1_0, h2_1_1); SB;
        const unsigned hv2_2 = pk2(h2_2_0, h2_2_1); SB;
        const unsigned hv2_3 = pk2(h2_3_0, h2_3_1); SB;
        *(u32x4*)(smem + rw + HOFF + 0) = u32x4{hv0_0, hv0_1, hv0_2, hv0_3}; SB;
        *(u32x4*)(smem + rw + HOFF + 1024) = u32x4{hv1_0, hv1_1, hv1_2, hv1_3}; SB;
        *(u32x4*)(smem + rw + HOFF + 2048) = u32x4{hv2_0, hv2_1, hv2_2, hv2_3}; SB;
      }
      STAMP(2);
      SB;
      tile4(IC<2>{}, cur_at(2));
      TS(6);
      BAR_LGKM();
      __builtin_amdgcn_s_setprio(P2_A);
      TS(7);
      STAMP(5);
      xl_one(ldsbase + w * XLS, cur_at(4 + w), true);
      {
        constexpr int XOFF = 16384, HOFF = hslot(1);
        u32x4* XN = XS1;
        const u32x4 cb0_0 = lds128(smem, xa[0][0] + XOFF + 0); SB;
        const u32x4 cb1_0 = lds128(smem, xa[0][1] + XOFF + 0); SB;
        const u32x4 cb0_1 = lds128(smem, xa[1][0] + XOFF + 0); SB;
        const u32x4 cb1_1 = lds128(smem, xa[1][1] + XOFF + 0); SB;
        const u32x4 cb0_2 = lds128(smem, xa[2][0] + XOFF + 0); SB;
        const u32x4 cb1_2 = lds128(smem, xa[2][1] + XOFF + 0); SB;
        const f32x4 xn0_0 = mfma16(CD[0], cb0_0, ZERO4); SB;
        const u32x4 cb0_3 = lds128(smem, xa[3][0] + XOFF + 0); SB;
        const u32x4 cb1_3 = lds128(smem, xa[3][1] + XOFF + 0); SB;
        const f32x4 xn1_0 = mfma16(CD[1], cb1_0, ZERO4); SB;
        const u32x4 cb0_4 = lds128(smem, xa[4][0] + XOFF + 0); SB;
        const u32x4 cb1_4 = lds128(smem, xa[4][1] + XOFF + 0); SB;
        const u32x4 cb2_0 = lds128(smem, xa[0][0] + XOFF + 64); SB;
        const u32x4 cb3_0 = lds128(smem, xa[0][1] + XOFF + 64); SB;
        const f32x4 xn0_1 = smf(CS[0], cb0_1, cb0_2, xn0_0, sidx); SB;
        const u32x4 cb2_1 = lds128(smem, xa[1][0] + XOFF + 64); SB;
        const f32x4 xn1_1 = smf(CS[2], cb1_1, cb1_2, xn1_0, sidx); SB;
        const u32x4 cb3_1 = lds128(smem, xa[1][1] + XOFF + 64); SB;
        const u32x4 cb2_2 = lds128(smem, xa[2][0] + XOFF + 64); SB;
        const u32x4 cb3_2 = lds128(smem, xa[2][1] + XOFF + 64); SB;
        const f32x4 xn0_2 = smf(CS[1], cb0_3, cb0_4, xn0_1, sidx); SB;
        const u32x4 cb2_3 = lds128(smem, xa[3][0] + XOFF + 64); SB;
        const f32x4 xn1_2 = smf(CS[3], cb1_3, cb1_4, xn1_1, sidx); SB;
        const u32x4 cb3_3 = lds128(smem, xa[3][1] + XOFF + 64); SB;
        const f32x4 xn2_0 = mfma16(CD[2], cb2_0, ZERO4); SB;
        const u32x4 cb2_4 = lds128(smem, xa[4][0] + XOFF + 64); SB;
        const f32x4 xn3_0 = mfma16(CD[3], cb3_0, ZERO4); SB;
        const u32x4 cb3_4 = lds128(smem, xa[4][1] + XOFF + 64); SB;
        const unsigned xb0_1 = pk2(xn0_2[2], xn0_2[3]); SB;
        const unsigned xb0_3 = pk2(xn1_2[2], xn1_2[3]); SB;
        const unsigned xb0_0 = pk2(xn0_2[0], xn0_2[1]); SB;
        const unsigned xb0_2 = pk2(xn1_2[0], xn1_2[1]); SB;
        const u32x4 xt0 = u32x4{xb0_0, xb0_1, xb0_2, xb0_3}; XN[0] = xt0;
        const u32x4 cb4_0 = lds128(smem, xa[0][0] + XOFF + 128); SB;
        const f32x4 s2_0 = mfma16(xt0, xt0, ZERO4); SB;
        const u32x4 cb5_0 = lds128(smem, xa[0][1] + XOFF + 128); SB;
        const f32x4 xn2_1 = smf(CS[4], cb2_1, cb2_2, xn2_0, sidx); SB;
        const u32x4 cb4_1 = lds128(smem, xa[1][0] + XOFF + 128); SB;
        const f32x4 xn3_1 = smf(CS[6], cb3_1, cb3_2, xn3_0, sidx); SB;
        const u32x4 cb5_1 = lds128(smem, xa[1][1] + XOFF + 128); SB;
        const f32x4 s1_0 = mfma16(ONES, xt0, ZERO4); SB;
        const u32x4 cb4_2 = lds128(smem, xa[2][0] + XOFF + 128); SB;
        const u32x4 cb5_2 = lds128(smem, xa[2][1] + XOFF + 128); SB;
        const f32x4 xn2_2 = smf(CS[5], cb2_3, cb2_4, xn2_1, sidx); SB;
        const u32x4 cb4_3 = lds128(smem, xa[3][0] + XOFF + 128); SB;
        const f32x4 xn3_2 = smf(CS[7], cb3_3, cb3_4, xn3_1, sidx); SB;
        const u32x4 cb5_3 = lds128(smem, xa[3][1] + XOFF + 128); SB;
        const u32x4 cb4_4 = lds128(smem, xa[4][0] + XOFF + 128); SB;
        const f32x4 xn4_0 = mfma16(CD[4], cb4_0, ZERO4); SB;
        const u32x4 cb5_4 = lds128(smem, xa[4][1] + XOFF + 128); SB;
        const unsigned xb1_1 = pk2(xn2_2[2], xn2_2[3]); SB;
        const unsigned xb1_0 = pk2(xn2_2[0], xn2_2[1]); SB;
        const unsigned xb1_2 = pk2(xn3_2[0], xn3_2[1]); SB;
        const unsigned xb1_3 = pk2(xn3_2[2], xn3_2[3]); SB;
        const u32x4 xt1 = u32x4{xb1_0, xb1_1, xb1_2, xb1_3}; XN[1] = xt1;
        const f32x4 xn5_0 = mfma16(CD[5], cb5_0, ZERO4); SB;
        const f32x4 xn5_1 = smf(CS[10], cb5_1, cb5_2, xn5_0, sidx); SB;
        const f32x4 xn4_1 = smf(CS[8], cb4_1, cb4_2, xn4_0, sidx); SB;
        const f32x4 s2_1 = mfma16(xt1, xt1, s2_0); SB;
        const f32x4 xn4_2 = smf(CS[9], cb4_3, cb4_4, xn4_1, sidx); SB;
        const f32x4 xn5_2 = smf(CS[11], cb5_3, cb5_4, xn5_1, sidx); SB;
        const f32x4 s1_1 = mfma16(ONES, xt1, s1_0); SB;
        const unsigned xb2_0 = pk2(xn4_2[0], xn4_2[1]); SB;
        const unsigned xb2_1 = pk2(xn4_2[2], xn4_2[3]); SB;
        const unsigned xb2_3 = pk2(xn5_2[2], xn5_2[3]); SB;
        const unsigned xb2_2 = pk2(xn5_2[0], xn5_2[1]); SB;
        const u32x4 xt2 = u32x4{xb2_0, xb2_1, xb2_2, xb2_3}; XN[2] = xt2;
        const f32x4 s2_2 = mfma16(xt2, xt2, s2_1); SB;
        const f32x4 s1_2 = mfma16(ONES, xt2, s1_1); SB;
        const float d0 = dm0 ? s2_2[0] : 0.0f; SB;
        const float d1 = dm1 ? s2_2[1] : d0; SB;
        const float d2 = dm2 ? s2_2[2] : d1; SB;
        const float d3 = dm3 ? s2_2[3] : d2; SB;
        const float mean = s1_2[0] * (1.0f / 96.0f); SB;
        const float tv = fmaf(-mean, mean, 1e-5f); SB;
        const f32x4 sq = __builtin_amdgcn_mfma_f32_16x16x4f32(1.0f, d3, ZERO4, 0, 0, 0); SB;
        const float var = fmaf(sq[0], 1.0f / 96.0f, tv); SB;
        const float varc = __builtin_amdgcn_fmed3f(var, 1e-5f, 3.0e38f); SB;
        const float rstd = __builtin_amdgcn_rsqf(varc); SB;
        const float nm = -mean * rstd; SB;
        const float h0_1_0 = fmaf(xn0_2[2], rstd, nm); SB;
        const float h2_3_0 = fmaf(xn5_2[2], rstd, nm); SB;
        const float h1_1_1 = fmaf(xn2_2[3], rstd, nm); SB;
        const float h1_0_0 = fmaf(xn2_2[0], rstd, nm); SB;
        const float h2_1_0 = fmaf(xn4_2[2], rstd, nm); SB;
        const float h1_0_1 = fmaf(xn2_2[1], rstd, nm); SB;
        const float h2_0_1 = fmaf(xn4_2[1], rstd, nm); SB;
        const float h0_1_1 = fmaf(xn0_2[3], rstd, nm); SB;
        const unsigned hv0_1 = pk2(h0_1_0, h0_1_1); SB;
        const float h0_3_0 = fmaf(xn1_2[2], rstd, nm); SB;
        __builtin_amdgcn_s_setprio(0); SB;
        const float h1_2_0 = fmaf(xn3_2[0], rstd, nm); SB;
        const float h1_3_0 = fmaf(xn3_2[2], rstd, nm); SB;
        const float h2_0_0 = fmaf(xn4_2[0], rstd, nm); SB;
        const float h0_0_0 = fmaf(xn0_2[0], rstd, nm); SB;
        const float h0_0_1 = fmaf(xn0_2[1], rstd, nm); SB;
        const float h2_2_0 = fmaf(xn5_2[0], rstd, nm); SB;
        const float h1_3_1 = fmaf(xn3_2[3], rstd, nm); SB;
        const float h2_2_1 = fmaf(xn5_2[1], rstd, nm); SB;
        const unsigned hv2_2 = pk2(h2_2_0, h2_2_1); SB;
        const float h0_3_1 = fmaf(xn1_2[3], rstd, nm); SB;
        const float h2_3_1 = fmaf(xn5_2[3], rstd, nm); SB;
        const float h0_2_1 = fmaf(xn1_2[1], rstd, nm); SB;
        const unsigned hv1_0 = pk2(h1_0_0, h1_0_1); SB;
        const float h1_1_0 = fmaf(xn2_2[2], rstd, nm); SB;
        const unsigned hv1_1 = pk2(h1_1_0, h1_1_1); SB;
        const float h0_2_0 = fmaf(xn1_2[0], rstd, nm); SB;
        const float h1_2_1 = fmaf(xn3_2[1], rstd, nm); SB;
        const float h2_1_1 = fmaf(xn4_2[3], rstd, nm); SB;
        const unsigned hv2_1 = pk2(h2_1_0, h2_1_1); SB;
        const unsigned hv0_3 = pk2(h0_3_0, h0_3_1); SB;
        const unsigned hv2_3 = pk2(h2_3_0, h2_3_1); SB;
        const unsigned hv2_0 = pk2(h2_0_0, h2_0_1); SB;
        const unsigned hv1_3 = pk2(h1_3_0, h1_3_1); SB;
        *(u32x4*)(smem + rw + HOFF + 2048) = u32x4{hv2_0, hv2_1, hv2_2, hv2_3}; SB;
        const unsigned hv0_0 = pk2(h0_0_0, h0_0_1); SB;
        const unsigned hv1_2 = pk2(h1_2_0, h1_2_1); SB;
        const unsigned hv0_2 = pk2(h0_2_0, h0_2_1); SB;
        *(u32x4*)(smem + rw + HOFF + 1024) = u32x4{hv1_0, hv1_1, hv1_2, hv1_3}; SB;
        *(u32x4*)(smem + rw + HOFF + 0) = u32x4{hv0_0, hv0_1, hv0_2, hv0_3}; SB;
      }
      STAMP(6);
      SB;
      {
        int ty, tx;
        tok(w, ty, tx);
        orel = (ty * IMG + tx) * PIXB + g * 16;
      }
#pragma unroll
      for (int k = 0; k < 3; ++k) { XS2[k] = u32x4{0u, 0u, 0u, 0u}; XS3[k] = XS2[k]; }
      asm volatile("" : "+v"(orel), "+v"(SEL0), "+v"(SEL1));
      TS(8);
      BAR_VM(0);
      TS(9);
      TSOUT();
      STAMP(1);
      __builtin_amdgcn_s_setprio(A_PRIO);
      int ocx = wstart % NWS;
      unsigned obase = (unsigned)(((wstart / NWS) * 7 * IMG + ocx * 7) * PIXB);
      obase -= ocx < PERX ? STEP_WRAP : STEP_IN;
      ocx += ocx < PERX ? NWS - PERX : -PERX;
      Cur ta = cur_at(4);
      auto dmaA = [&](auto SC, int n) {
        constexpr int SLOT = decltype(SC)::value;
        const unsigned inb = ta.base - (unsigned)((IMG + 1) * PIXB);
        u32x4 srd = xsrd;
        srd[2] = n + 4 < nwin ? XBYTES : 0u;
        const unsigned dst = ldsbase + XB + w * 4096;
        const bool lr = (ta.cx == 0) | (ta.cx == NWS - 1);
        const bool tb = (ta.cy == 0) | (ta.cy == NWS - 1);
        if (__builtin_expect(lr, 0)) {
          const unsigned ln = lane_id_opaque();
          dma16x<SLOT * 16384 + 0>(dst, (rel4[0] + inb) | tile_cut((w * 4 + 0) * 64 + ln, ta.cx == 0, ta.cx == NWS - 1), srd, 0u);
          dma16x<SLOT * 16384 + 1024>(dst, (rel4[1] + inb) | tile_cut(tile_src((w * 4 + 1) * 64 + ln, true), ta.cx == 0, ta.cx == NWS - 1), srd, 0u);
          dma16x<SLOT * 16384 + 2048>(dst, (rel4[2] + inb) | tile_cut((w * 4 + 2) * 64 + ln, ta.cx == 0, ta.cx == NWS - 1), srd, 0u);
          dma16x<SLOT * 16384 + 3072>(dst, (rel4[3] + inb) | tile_cut((w * 4 + 3) * 64 + ln, ta.cx == 0, ta.cx == NWS - 1), srd, 0u);
        } else if (__builtin_expect(tb, 0)) {
          dma16x<SLOT * 16384 + 0>(dst, rel4[0] + inb, srd, 0u);
          dma16x<SLOT * 16384 + 1024>(dst, rel4[1] + inb, srd, 0u);
          dma16x<SLOT * 16384 + 2048>(dst, rel4[2] + inb, srd, 0u);
          dma16x<SLOT * 16384 + 3072>(dst, rel4[3] + inb, srd, 0u);
        } else {
          dma16x<SLOT * 16384 + 0>(dst, rel4[0], srd, inb);
          dma16x<SLOT * 16384 + 1024>(dst, rel4[1], srd, inb);
          dma16x<SLOT * 16384 + 2048>(dst, rel4[2], srd, inb);
          dma16x<SLOT * 16384 + 3072>(dst, rel4[3], srd, inb);
        }
      };
      auto stepA = [&](auto VC, int n) {
        constexpr int V = decltype(VC)::value;
        constexpr int XOFF = ((V + 2) & 3) * 16384;
        constexpr int HOFF = hslot((V + 2) & 3);
        constexpr int OOFF = OR_ - H1 + ((V + 1) & 1) * 9216;
        constexpr int QOFF = (V & 1) * 3072;
        u32x4* XP = V == 0 ? XS3 : (V == 1 ? XS0 : (V == 2 ? XS1 : XS2));
        u32x4* XC = V == 0 ? XS0 : (V == 1 ? XS1 : (V == 2 ? XS2 : XS3));
        u32x4* XN = V == 0 ? XS2 : (V == 1 ? XS3 : (V == 2 ? XS0 : XS1));
        const __amdgpu_buffer_rsrc_t psrd = V == 0 ? __builtin_amdgcn_make_buffer_rsrc(out, 0, n > 0 ? XBYTES : 0u, 0x00020000) : osrd;
        SB;
        if constexpr (V == 0) {
        __builtin_amdgcn_s_setprio(2); SB;
        const f32x4 pi0 = mfma16(SEL0, XP[0], ZERO4); SB;
        const u32x4 cb0_0 = lds128(smem, xa[0][0] + XOFF + 0); SB;
        const u32x4 cb1_0 = lds128(smem, xa[0][1] + XOFF + 0); SB;
        const f32x4 pi1 = mfma16(SEL1, XP[0], ZERO4); SB;
        const u32x4 cb0_1 = lds128(smem, xa[1][0] + XOFF + 0); SB;
        const u32x4 cb1_1 = lds128(smem, xa[1][1] + XOFF + 0); SB;
        const f32x4 pi2 = mfma16(SEL0, XP[1], ZERO4); SB;
        const u32x4 cb0_2 = lds128(smem, xa[2][0] + XOFF + 0); SB;
        const u32x4 cb1_2 = lds128(smem, xa[2][1] + XOFF + 0); SB;
        const f32x4 pi3 = mfma16(SEL1, XP[1], ZERO4); SB;
        const u32x4 cb0_3 = lds128(smem, xa[3][0] + XOFF + 0); SB;
        const u32x4 cb1_3 = lds128(smem, xa[3][1] + XOFF + 0); SB;
        const f32x4 xn0_0 = mfma16(CD[0], cb0_0, ZERO4); SB;
        const u32x4 cb0_4 = lds128(smem, xa[4][0] + XOFF + 0); SB;
        const u32x4 cb1_4 = lds128(smem, xa[4][1] + XOFF + 0); SB;
        const f32x4 xn0_1 = smf(CS[0], cb0_1, cb0_2, xn0_0, sidx); SB;
        const f32x4 xn1_0 = mfma16(CD[1], cb1_0, ZERO4); SB;
        const f32x4 xn1_1 = smf(CS[2], cb1_1, cb1_2, xn1_0, sidx); SB;
        const u32x4 cb2_0 = lds128(smem, xa[0][0] + XOFF + 64); SB;
        const f32x4 xn0_2 = smf(CS[1], cb0_3, cb0_4, xn0_1, sidx); SB;
        const f32x4 xn1_2 = smf(CS[3], cb1_3, cb1_4, xn1_1, sidx); SB;
        const u32x4 cb3_0 = lds128(smem, xa[0][1] + XOFF + 64); SB;
        const u32x4 cb2_1 = lds128(smem, xa[1][0] + XOFF + 64); SB;
        const u32x4 cb3_1 = lds128(smem, xa[1][1] + XOFF + 64); SB;
        const u32x4 cb2_2 = lds128(smem, xa[2][0] + XOFF + 64); SB;
        const u32x4 cb3_2 = lds128(smem, xa[2][1] + XOFF + 64); SB;
        const f32x4 xn2_0 = mfma16(CD[2], cb2_0, ZERO4); SB;
        const unsigned xb0_0 = pk2(xn0_2[0], xn0_2[1]); SB;
        const unsigned xb0_1 = pk2(xn0_2[2], xn0_2[3]); SB;
        const unsigned xb0_2 = pk2(xn1_2[0], xn1_2[1]); SB;
        const unsigned xb0_3 = pk2(xn1_2[2], xn1_2[3]); SB;
        const u32x4 xt0 = u32x4{xb0_0, xb0_1, xb0_2, xb0_3}; XN[0] = xt0;
        const u32x4 cb2_3 = lds128(smem, xa[3][0] + XOFF + 64); SB;
        const f32x4 xn3_0 = mfma16(CD[3], cb3_0, ZERO4); SB;
        const u32x4 cb3_3 = lds128(smem, xa[3][1] + XOFF + 64); SB;
        const f32x4 s2_0 = mfma16(xt0, xt0, ZERO4); SB;
        const u32x4 cb2_4 = lds128(smem, xa[4][0] + XOFF + 64); SB;
        const f32x4 s1_0 = mfma16(ONES, xt0, ZERO4); SB;
        const u32x4 cb3_4 = lds128(smem, xa[4][1] + XOFF + 64); SB;
        const u32x4 cb4_0 = lds128(smem, xa[0][0] + XOFF + 128); SB;
        const f32x4 xn2_1 = smf(CS[4], cb2_1, cb2_2, xn2_0, sidx); SB;
        const f32x4 xn3_1 = smf(CS[6], cb3_1, cb3_2, xn3_0, sidx); SB;
        const u32x4 cb5_0 = lds128(smem, xa[0][1] + XOFF + 128); SB;
        const u32x4 cb4_1 = lds128(smem, xa[1][0] + XOFF + 128); SB;
        const u32x4 cb5_1 = lds128(smem, xa[1][1] + XOFF + 128); SB;
        const f32x4 xn3_2 = smf(CS[7], cb3_3, cb3_4, xn3_1, sidx); SB;
        const u32x4 cb4_2 = lds128(smem, xa[2][0] + XOFF + 128); SB;
        const u32x4 cb5_2 = lds128(smem, xa[2][1] + XOFF + 128); SB;
        const u32x4 cb4_3 = lds128(smem, xa[3][0] + XOFF + 128); SB;
        const u32x4 cb5_3 = lds128(smem, xa[3][1] + XOFF + 128); SB;
        const f32x4 xn4_0 = mfma16(CD[4], cb4_0, ZERO4); SB;
        const u32x4 cb4_4 = lds128(smem, xa[4][0] + XOFF + 128); SB;
        const f32x4 xn2_2 = smf(CS[5], cb2_3, cb2_4, xn2_1, sidx); SB;
        const unsigned xb1_0 = pk2(xn2_2[0], xn2_2[1]); SB;
        const unsigned xb1_1 = pk2(xn2_2[2], xn2_2[3]); SB;
        const unsigned xb1_2 = pk2(xn3_2[0], xn3_2[1]); SB;
        const unsigned xb1_3 = pk2(xn3_2[2], xn3_2[3]); SB;
        const u32x4 xt1 = u32x4{xb1_0, xb1_1, xb1_2, xb1_3}; XN[1] = xt1;
        const f32x4 s2_1 = mfma16(xt1, xt1, s2_0); SB;
        const u32x4 of0 = lds128(smem, rw + OOFF + 0); SB;
        const f32x4 s1_1 = mfma16(ONES, xt1, s1_0); SB;
        const u32x4 cb5_4 = lds128(smem, xa[4][1] + XOFF + 128); SB;
        *(u32x4*)(smem + rq + QOFF) = XC[2]; SB;
        const f32x4 xn5_0 = mfma16(CD[5], cb5_0, ZERO4); SB;
        const f32x4 xn5_1 = smf(CS[10], cb5_1, cb5_2, xn5_0, sidx); SB;
        const u32x4 wo0_0 = lds128(smem, wob + 0); SB;
        const u32x4 wo0_1 = lds128(smem, wob + 1024); SB;
        const f32x4 xn5_2 = smf(CS[11], cb5_3, cb5_4, xn5_1, sidx); SB;
        const u32x4 wo0_2 = lds128(smem, wob + 2048); SB;
        const u32x4 wo1_0 = lds128(smem, wob + 3072); SB;
        const unsigned xb2_2 = pk2(xn5_2[0], xn5_2[1]); SB;
        const unsigned xb2_3 = pk2(xn5_2[2], xn5_2[3]); SB;
        const f32x4 xn4_1 = smf(CS[8], cb4_1, cb4_2, xn4_0, sidx); SB;
        const f32x4 xn4_2 = smf(CS[9], cb4_3, cb4_4, xn4_1, sidx); SB;
        const u32x4 of1 = lds128(smem, rw + OOFF + 1024); SB;
        const u32x4 of2 = lds128(smem, rw + OOFF + 2048); SB;
        const unsigned xb2_0 = pk2(xn4_2[0], xn4_2[1]); SB;
        const unsigned xb2_1 = pk2(xn4_2[2], xn4_2[3]); SB;
        const u32x4 xt2 = u32x4{xb2_0, xb2_1, xb2_2, xb2_3}; XN[2] = xt2;
        const f32x4 s2_2 = mfma16(xt2, xt2, s2_1); SB;
        const f32x4 s1_2 = mfma16(ONES, xt2, s1_1); SB;
        const f32x4 p0_0 = mfma16(wo0_0, of0, pi0); SB;
        const u32x4 wo1_1 = lds128(smem, wob + 4096); SB;
        const f32x4 p1_0 = mfma16(wo1_0, of0, pi1); SB;
        const u32x4 wo1_2 = lds128(smem, wob + 5120); SB;
        const f32x4 p0_1 = mfma16(wo0_1, of1, p0_0); SB;
        const u32x4 wo2_0 = lds128(smem, wob + 6144); SB;
        const f32x4 p0_2 = mfma16(wo0_2, of2, p0_1); SB;
        const u32x4 wo2_1 = lds128(smem, wob + 7168); SB;
        const float d0 = dm0 ? s2_2[0] : 0.0f; SB;
        const float d1 = dm1 ? s2_2[1] : d0; SB;
        const float d2 = dm2 ? s2_2[2] : d1; SB;
        const float d3 = dm3 ? s2_2[3] : d2; SB;
        const f32x4 sq = __builtin_amdgcn_mfma_f32_16x16x4f32(1.0f, d3, ZERO4, 0, 0, 0); SB;
        const u32x4 wo2_2 = lds128(smem, wob + 8192); SB;
        const unsigned po0_0 = pk2(p0_2[0], p0_2[1]); SB;
        const float mean = s1_2[0] * (1.0f / 96.0f); SB;
        const float tv = fmaf(-mean, mean, 1e-5f); SB;
        const unsigned po0_1 = pk2(p0_2[2], p0_2[3]); SB;
        const f32x4 p2_0 = mfma16(wo2_0, of0, pi2); SB;
        const u32x4 wo3_0 = lds128(smem, wob + 9216); SB;
        const u32x4 wo3_1 = lds128(smem, wob + 10240); SB;
        const f32x4 p2_1 = mfma16(wo2_1, of1, p2_0); SB;
        const u32x4 wo3_2 = lds128(smem, wob + 11264); SB;
        const float var = fmaf(sq[0], 1.0f / 96.0f, tv); SB;
        const float varc = __builtin_amdgcn_fmed3f(var, 1e-5f, 3.0e38f); SB;
        const float rstd = __builtin_amdgcn_rsqf(varc); SB;
        const f32x4 p2_2 = mfma16(wo2_2, of2, p2_1); SB;
        const float nm = -mean * rstd; SB;
        const float h0_1_1 = fmaf(xn0_2[3], rstd, nm); SB;
        const float h0_2_0 = fmaf(xn1_2[0], rstd, nm); SB;
        const f32x4 p1_1 = mfma16(wo1_1, of1, p1_0); SB;
        const f32x4 p1_2 = mfma16(wo1_2, of2, p1_1); SB;
        const float h0_1_0 = fmaf(xn0_2[2], rstd, nm); SB;
        const float h0_2_1 = fmaf(xn1_2[1], rstd, nm); SB;
        __builtin_amdgcn_s_setprio(0); SB;
        const float h0_3_0 = fmaf(xn1_2[2], rstd, nm); SB;
        const float h0_3_1 = fmaf(xn1_2[3], rstd, nm); SB;
        const float h1_0_0 = fmaf(xn2_2[0], rstd, nm); SB;
        const float h0_0_0 = fmaf(xn0_2[0], rstd, nm); SB;
        const float h0_0_1 = fmaf(xn0_2[1], rstd, nm); SB;
        const unsigned hv0_0 = pk2(h0_0_0, h0_0_1); SB;
        const float h1_0_1 = fmaf(xn2_2[1], rstd, nm); SB;
        const float h1_1_0 = fmaf(xn2_2[2], rstd, nm); SB;
        const float h1_1_1 = fmaf(xn2_2[3], rstd, nm); SB;
        const float h2_0_0 = fmaf(xn4_2[0], rstd, nm); SB;
        const float h1_2_0 = fmaf(xn3_2[0], rstd, nm); SB;
        const float h2_0_1 = fmaf(xn4_2[1], rstd, nm); SB;
        const float h1_2_1 = fmaf(xn3_2[1], rstd, nm); SB;
        const float h1_3_0 = fmaf(xn3_2[2], rstd, nm); SB;
        const unsigned hv0_1 = pk2(h0_1_0, h0_1_1); SB;
        const unsigned hv0_2 = pk2(h0_2_0, h0_2_1); SB;
        const unsigned hv1_1 = pk2(h1_1_0, h1_1_1); SB;
        const unsigned hv1_0 = pk2(h1_0_0, h1_0_1); SB;
        const float h2_1_1 = fmaf(xn4_2[3], rstd, nm); SB;
        const unsigned hv2_0 = pk2(h2_0_0, h2_0_1); SB;
        const float h1_3_1 = fmaf(xn3_2[3], rstd, nm); SB;
        const float h2_1_0 = fmaf(xn4_2[2], rstd, nm); SB;
        const float h2_2_1 = fmaf(xn5_2[1], rstd, nm); SB;
        const float h2_3_0 = fmaf(xn5_2[2], rstd, nm); SB;
        const float h2_3_1 = fmaf(xn5_2[3], rstd, nm); SB;
        const unsigned hv0_3 = pk2(h0_3_0, h0_3_1); SB;
        const unsigned hv2_1 = pk2(h2_1_0, h2_1_1); SB;
        const float h2_2_0 = fmaf(xn5_2[0], rstd, nm); SB;
        const unsigned hv2_2 = pk2(h2_2_0, h2_2_1); SB;
        const unsigned hv2_3 = pk2(h2_3_0, h2_3_1); SB;
        *(u32x4*)(smem + rw + HOFF + 2048) = u32x4{hv2_0, hv2_1, hv2_2, hv2_3}; SB;
        const unsigned po1_0 = pk2(p1_2[0], p1_2[1]); SB;
        const unsigned po1_1 = pk2(p1_2[2], p1_2[3]); SB;
        __builtin_amdgcn_raw_buffer_store_b128(u32x4{po0_0, po0_1, po1_0, po1_1}, psrd, orel, obase + 0, ST_AUX); SB;
        const unsigned po2_0 = pk2(p2_2[0], p2_2[1]); SB;
        const unsigned po2_1 = pk2(p2_2[2], p2_2[3]); SB;
        *(u32x4*)(smem + rw + HOFF + 0) = u32x4{hv0_0, hv0_1, hv0_2, hv0_3}; SB;
        const f32x4 p3_0 = mfma16(wo3_0, of0, pi3); SB;
        const f32x4 p3_1 = mfma16(wo3_1, of1, p3_0); SB;
        const f32x4 p3_2 = mfma16(wo3_2, of2, p3_1); SB;
        const unsigned po3_0 = pk2(p3_2[0], p3_2[1]); SB;
        const unsigned hv1_2 = pk2(h1_2_0, h1_2_1); SB;
        const unsigned hv1_3 = pk2(h1_3_0, h1_3_1); SB;
        *(u32x4*)(smem + rw + HOFF + 1024) = u32x4{hv1_0, hv1_1, hv1_2, hv1_3}; SB;
        const unsigned po3_1 = pk2(p3_2[2], p3_2[3]); SB;
        __builtin_amdgcn_raw_buffer_store_b128(u32x4{po2_0, po2_1, po3_0, po3_1}, psrd, orel, obase + 64, ST_AUX); SB;
        } else if constexpr (V == 1) {
        __builtin_amdgcn_s_setprio(2); SB;
        const f32x4 pi0 = mfma16(SEL0, XP[0], ZERO4); SB;
        const u32x4 cb0_0 = lds128(smem, xa[0][0] + XOFF + 0); SB;
        const u32x4 cb1_0 = lds128(smem, xa[0][1] + XOFF + 0); SB;
        const f32x4 pi1 = mfma16(SEL1, XP[0], ZERO4); SB;
        const u32x4 cb0_1 = lds128(smem, xa[1][0] + XOFF + 0); SB;
        const u32x4 cb1_1 = lds128(smem, xa[1][1] + XOFF + 0); SB;
        const f32x4 pi2 = mfma16(SEL0, XP[1], ZERO4); SB;
        const u32x4 cb0_2 = lds128(smem, xa[2][0] + XOFF + 0); SB;
        const f32x4 pi3 = mfma16(SEL1, XP[1], ZERO4); SB;
        const u32x4 cb1_2 = lds128(smem, xa[2][1] + XOFF + 0); SB;
        const u32x4 cb0_3 = lds128(smem, xa[3][0] + XOFF + 0); SB;
        const u32x4 cb1_3 = lds128(smem, xa[3][1] + XOFF + 0); SB;
        const f32x4 xn0_0 = mfma16(CD[0], cb0_0, ZERO4); SB;
        const f32x4 xn1_0 = mfma16(CD[1], cb1_0, ZERO4); SB;
        const u32x4 cb0_4 = lds128(smem, xa[4][0] + XOFF + 0); SB;
        const u32x4 cb1_4 = lds128(smem, xa[4][1] + XOFF + 0); SB;
        const f32x4 xn0_1 = smf(CS[0], cb0_1, cb0_2, xn0_0, sidx); SB;
        const f32x4 xn1_1 = smf(CS[2], cb1_1, cb1_2, xn1_0, sidx); SB;
        const u32x4 cb2_0 = lds128(smem, xa[0][0] + XOFF + 64); SB;
        const u32x4 cb3_0 = lds128(smem, xa[0][1] + XOFF + 64); SB;
        const u32x4 cb2_1 = lds128(smem, xa[1][0] + XOFF + 64); SB;
        const u32x4 cb3_1 = lds128(smem, xa[1][1] + XOFF + 64); SB;
        const f32x4 xn0_2 = smf(CS[1], cb0_3, cb0_4, xn0_1, sidx); SB;
        const f32x4 xn1_2 = smf(CS[3], cb1_3, cb1_4, xn1_1, sidx); SB;
        const u32x4 cb2_2 = lds128(smem, xa[2][0] + XOFF + 64); SB;
        const unsigned xb0_1 = pk2(xn0_2[2], xn0_2[3]); SB;
        const u32x4 cb3_2 = lds128(smem, xa[2][1] + XOFF + 64); SB;
        const f32x4 xn2_0 = mfma16(CD[2], cb2_0, ZERO4); SB;
        const unsigned xb0_0 = pk2(xn0_2[0], xn0_2[1]); SB;
        const unsigned xb0_2 = pk2(xn1_2[0], xn1_2[1]); SB;
        const f32x4 xn2_1 = smf(CS[4], cb2_1, cb2_2, xn2_0, sidx); SB;
        const unsigned xb0_3 = pk2(xn1_2[2], xn1_2[3]); SB;
        const u32x4 xt0 = u32x4{xb0_0, xb0_1, xb0_2, xb0_3}; XN[0] = xt0;
        const u32x4 cb2_3 = lds128(smem, xa[3][0] + XOFF + 64); SB;
        const f32x4 xn3_0 = mfma16(CD[3], cb3_0, ZERO4); SB;
        const u32x4 cb3_3 = lds128(smem, xa[3][1] + XOFF + 64); SB;
        const u32x4 cb2_4 = lds128(smem, xa[4][0] + XOFF + 64); SB;
        const u32x4 cb3_4 = lds128(smem, xa[4][1] + XOFF + 64); SB;
        const u32x4 cb4_0 = lds128(smem, xa[0][0] + XOFF + 128); SB;
        const f32x4 s1_0 = mfma16(ONES, xt0, ZERO4); SB;
        const f32x4 s2_0 = mfma16(xt0, xt0, ZERO4); SB;
        const f32x4 xn3_1 = smf(CS[6], cb3_1, cb3_2, xn3_0, sidx); SB;
        const f32x4 xn3_2 = smf(CS[7], cb3_3, cb3_4, xn3_1, sidx); SB;
        const u32x4 cb5_0 = lds128(smem, xa[0][1] + XOFF + 128); SB;
        const u32x4 cb4_1 = lds128(smem, xa[1][0] + XOFF + 128); SB;
        const u32x4 cb5_1 = lds128(smem, xa[1][1] + XOFF + 128); SB;
        const u32x4 cb4_2 = lds128(smem, xa[2][0] + XOFF + 128); SB;
        const f32x4 xn2_2 = smf(CS[5], cb2_3, cb2_4, xn2_1, sidx); SB;
        const u32x4 cb5_2 = lds128(smem, xa[2][1] + XOFF + 128); SB;
        const f32x4 xn4_0 = mfma16(CD[4], cb4_0, ZERO4); SB;
        const u32x4 cb4_3 = lds128(smem, xa[3][0] + XOFF + 128); SB;
        const u32x4 cb5_3 = lds128(smem, xa[3][1] + XOFF + 128); SB;
        const u32x4 cb4_4 = lds128(smem, xa[4][0] + XOFF + 128); SB;
        const unsigned xb1_0 = pk2(xn2_2[0], xn2_2[1]); SB;
        const unsigned xb1_2 = pk2(xn3_2[0], xn3_2[1]); SB;
        const unsigned xb1_3 = pk2(xn3_2[2], xn3_2[3]); SB;
        const unsigned xb1_1 = pk2(xn2_2[2], xn2_2[3]); SB;
        const u32x4 xt1 = u32x4{xb1_0, xb1_1, xb1_2, xb1_3}; XN[1] = xt1;
        const f32x4 s2_1 = mfma16(xt1, xt1, s2_0); SB;
        const u32x4 of0 = lds128(smem, rw + OOFF + 0); SB;
        const u32x4 cb5_4 = lds128(smem, xa[4][1] + XOFF + 128); SB;
        const f32x4 s1_1 = mfma16(ONES, xt1, s1_0); SB;
        const f32x4 xn4_1 = smf(CS[8], cb4_1, cb4_2, xn4_0, sidx); SB;
        *(u32x4*)(smem + rq + QOFF) = XC[2]; SB;
        const f32x4 xn4_2 = smf(CS[9], cb4_3, cb4_4, xn4_1, sidx); SB;
        const u32x4 of2 = lds128(smem, rw + OOFF + 2048); SB;
        const f32x4 xn5_0 = mfma16(CD[5], cb5_0, ZERO4); SB;
        const f32x4 xn5_1 = smf(CS[10], cb5_1, cb5_2, xn5_0, sidx); SB;
        const unsigned xb2_0 = pk2(xn4_2[0], xn4_2[1]); SB;
        const unsigned xb2_1 = pk2(xn4_2[2], xn4_2[3]); SB;
        const u32x4 wo0_0 = lds128(smem, wob + 0); SB;
        const u32x4 of1 = lds128(smem, rw + OOFF + 1024); SB;
        const u32x4 wo0_1 = lds128(smem, wob + 1024); SB;
        const f32x4 xn5_2 = smf(CS[11], cb5_3, cb5_4, xn5_1, sidx); SB;
        const unsigned xb2_2 = pk2(xn5_2[0], xn5_2[1]); SB;
        const unsigned xb2_3 = pk2(xn5_2[2], xn5_2[3]); SB;
        const u32x4 xt2 = u32x4{xb2_0, xb2_1, xb2_2, xb2_3}; XN[2] = xt2;
        const f32x4 s2_2 = mfma16(xt2, xt2, s2_1); SB;
        const u32x4 wo0_2 = lds128(smem, wob + 2048); SB;
        const u32x4 wo1_0 = lds128(smem, wob + 3072); SB;
        const f32x4 s1_2 = mfma16(ONES, xt2, s1_1); SB;
        const f32x4 p0_0 = mfma16(wo0_0, of0, pi0); SB;
        const u32x4 wo1_1 = lds128(smem, wob + 4096); SB;
        const u32x4 wo1_2 = lds128(smem, wob + 5120); SB;
        const f32x4 p0_1 = mfma16(wo0_1, of1, p0_0); SB;
        const f32x4 p1_0 = mfma16(wo1_0, of0, pi1); SB;
        const u32x4 wo2_0 = lds128(smem, wob + 6144); SB;
        const float d0 = dm0 ? s2_2[0] : 0.0f; SB;
        const f32x4 p0_2 = mfma16(wo0_2, of2, p0_1); SB;
        const u32x4 wo2_1 = lds128(smem, wob + 7168); SB;
        const float d1 = dm1 ? s2_2[1] : d0; SB;
        const float d2 = dm2 ? s2_2[2] : d1; SB;
        const float d3 = dm3 ? s2_2[3] : d2; SB;
        const float mean = s1_2[0] * (1.0f / 96.0f); SB;
        const float tv = fmaf(-mean, mean, 1e-5f); SB;
        const f32x4 sq = __builtin_amdgcn_mfma_f32_16x16x4f32(1.0f, d3, ZERO4, 0, 0, 0); SB;
        const u32x4 wo2_2 = lds128(smem, wob + 8192); SB;
        const float var = fmaf(sq[0], 1.0f / 96.0f, tv); SB;
        const float varc = __builtin_amdgcn_fmed3f(var, 1e-5f, 3.0e38f); SB;
        const unsigned po0_1 = pk2(p0_2[2], p0_2[3]); SB;
        const u32x4 wo3_0 = lds128(smem, wob + 9216); SB;
        const f32x4 p1_1 = mfma16(wo1_1, of1, p1_0); SB;
        const f32x4 p1_2 = mfma16(wo1_2, of2, p1_1); SB;
        const float rstd = __builtin_amdgcn_rsqf(varc); SB;
        const unsigned po0_0 = pk2(p0_2[0], p0_2[1]); SB;
        const f32x4 p2_0 = mfma16(wo2_0, of0, pi2); SB;
        const u32x4 wo3_1 = lds128(smem, wob + 10240); SB;
        const u32x4 wo3_2 = lds128(smem, wob + 11264); SB;
        const f32x4 p2_1 = mfma16(wo2_1, of1, p2_0); SB;
        const float nm = -mean * rstd; SB;
        const float h0_2_1 = fmaf(xn1_2[1], rstd, nm); SB;
        const float h0_3_0 = fmaf(xn1_2[2], rstd, nm); SB;
        const float h1_0_1 = fmaf(xn2_2[1], rstd, nm); SB;
        const float h1_1_0 = fmaf(xn2_2[2], rstd, nm); SB;
        const float h1_1_1 = fmaf(xn2_2[3], rstd, nm); SB;
        const float h1_2_0 = fmaf(xn3_2[0], rstd, nm); SB;
        __builtin_amdgcn_s_setprio(1); SB;
        const float h0_3_1 = fmaf(xn1_2[3], rstd, nm); SB;
        const float h1_0_0 = fmaf(xn2_2[0], rstd, nm); SB;
        const float h1_3_0 = fmaf(xn3_2[2], rstd, nm); SB;
        const float h0_0_0 = fmaf(xn0_2[0], rstd, nm); SB;
        const float h0_1_1 = fmaf(xn0_2[3], rstd, nm); SB;
        const float h0_2_0 = fmaf(xn1_2[0], rstd, nm); SB;
        const float h1_3_1 = fmaf(xn3_2[3], rstd, nm); SB;
        const f32x4 p2_2 = mfma16(wo2_2, of2, p2_1); SB;
        const float h2_0_0 = fmaf(xn4_2[0], rstd, nm); SB;
        const float h2_0_1 = fmaf(xn4_2[1], rstd, nm); SB;
        const float h2_1_0 = fmaf(xn4_2[2], rstd, nm); SB;
        const float h2_1_1 = fmaf(xn4_2[3], rstd, nm); SB;
        const float h2_2_0 = fmaf(xn5_2[0], rstd, nm); SB;
        const float h2_2_1 = fmaf(xn5_2[1], rstd, nm); SB;
        const unsigned hv2_1 = pk2(h2_1_0, h2_1_1); SB;
        const unsigned hv1_1 = pk2(h1_1_0, h1_1_1); SB;
        const float h1_2_1 = fmaf(xn3_2[1], rstd, nm); SB;
        const unsigned hv1_2 = pk2(h1_2_0, h1_2_1); SB;
        const unsigned hv2_0 = pk2(h2_0_0, h2_0_1); SB;
        const unsigned hv2_2 = pk2(h2_2_0, h2_2_1); SB;
        const float h2_3_1 = fmaf(xn5_2[3], rstd, nm); SB;
        const float h2_3_0 = fmaf(xn5_2[2], rstd, nm); SB;
        const float h0_0_1 = fmaf(xn0_2[1], rstd, nm); SB;
        const float h0_1_0 = fmaf(xn0_2[2], rstd, nm); SB;
        const unsigned hv0_0 = pk2(h0_0_0, h0_0_1); SB;
        const unsigned hv0_3 = pk2(h0_3_0, h0_3_1); SB;
        const unsigned hv1_0 = pk2(h1_0_0, h1_0_1); SB;
        const unsigned po1_0 = pk2(p1_2[0], p1_2[1]); SB;
        const unsigned hv2_3 = pk2(h2_3_0, h2_3_1); SB;
        const unsigned hv0_2 = pk2(h0_2_0, h0_2_1); SB;
        const unsigned po1_1 = pk2(p1_2[2], p1_2[3]); SB;
        __builtin_amdgcn_raw_buffer_store_b128(u32x4{po0_0, po0_1, po1_0, po1_1}, psrd, orel, obase + 0, ST_AUX); SB;
        const unsigned hv0_1 = pk2(h0_1_0, h0_1_1); SB;
        *(u32x4*)(smem + rw + HOFF + 0) = u32x4{hv0_0, hv0_1, hv0_2, hv0_3}; SB;
        const unsigned hv1_3 = pk2(h1_3_0, h1_3_1); SB;
        *(u32x4*)(smem + rw + HOFF + 1024) = u32x4{hv1_0, hv1_1, hv1_2, hv1_3}; SB;
        const unsigned po2_0 = pk2(p2_2[0], p2_2[1]); SB;
        const unsigned po2_1 = pk2(p2_2[2], p2_2[3]); SB;
        const f32x4 p3_0 = mfma16(wo3_0, of0, pi3); SB;
        const f32x4 p3_1 = mfma16(wo3_1, of1, p3_0); SB;
        const f32x4 p3_2 = mfma16(wo3_2, of2, p3_1); SB;
        *(u32x4*)(smem + rw + HOFF + 2048) = u32x4{hv2_0, hv2_1, hv2_2, hv2_3}; SB;
        const unsigned po3_0 = pk2(p3_2[0], p3_2[1]); SB;
        const unsigned po3_1 = pk2(p3_2[2], p3_2[3]); SB;
        __builtin_amdgcn_raw_buffer_store_b128(u32x4{po2_0, po2_1, po3_0, po3_1}, psrd, orel, obase + 64, ST_AUX); SB;
        } else if constexpr (V == 2) {
        __builtin_amdgcn_s_setprio(2); SB;
        const f32x4 pi0 = mfma16(SEL0, XP[0], ZERO4); SB;
        const u32x4 cb0_0 = lds128(smem, xa[0][0] + XOFF + 0); SB;
        const u32x4 cb1_0 = lds128(smem, xa[0][1] + XOFF + 0); SB;
        const f32x4 pi1 = mfma16(SEL1, XP[0], ZERO4); SB;
        const u32x4 cb0_1 = lds128(smem, xa[1][0] + XOFF + 0); SB;
        const f32x4 pi3 = mfma16(SEL1, XP[1], ZERO4); SB;
        const u32x4 cb1_1 = lds128(smem, xa[1][1] + XOFF + 0); SB;
        const f32x4 pi2 = mfma16(SEL0, XP[1], ZERO4); SB;
        const u32x4 cb0_2 = lds128(smem, xa[2][0] + XOFF + 0); SB;
        const u32x4 cb1_2 = lds128(smem, xa[2][1] + XOFF + 0); SB;
        const u32x4 cb0_3 = lds128(smem, xa[3][0] + XOFF + 0); SB;
        const u32x4 cb1_3 = lds128(smem, xa[3][1] + XOFF + 0); SB;
        const f32x4 xn0_0 = mfma16(CD[0], cb0_0, ZERO4); SB;
        const f32x4 xn1_0 = mfma16(CD[1], cb1_0, ZERO4); SB;
        const u32x4 cb0_4 = lds128(smem, xa[4][0] + XOFF + 0); SB;
        const u32x4 cb1_4 = lds128(smem, xa[4][1] + XOFF + 0); SB;
        const f32x4 xn0_1 = smf(CS[0], cb0_1, cb0_2, xn0_0, sidx); SB;
        const f32x4 xn1_1 = smf(CS[2], cb1_1, cb1_2, xn1_0, sidx); SB;
        const u32x4 cb2_0 = lds128(smem, xa[0][0] + XOFF + 64); SB;
        const u32x4 cb3_0 = lds128(smem, xa[0][1] + XOFF + 64); SB;
        const f32x4 xn0_2 = smf(CS[1], cb0_3, cb0_4, xn0_1, sidx); SB;
        const unsigned xb0_1 = pk2(xn0_2[2], xn0_2[3]); SB;
        const u32x4 cb2_1 = lds128(smem, xa[1][0] + XOFF + 64); SB;
        const u32x4 cb3_1 = lds128(smem, xa[1][1] + XOFF + 64); SB;
        const f32x4 xn1_2 = smf(CS[3], cb1_3, cb1_4, xn1_1, sidx); SB;
        const u32x4 cb2_2 = lds128(smem, xa[2][0] + XOFF + 64); SB;
        const u32x4 cb3_2 = lds128(smem, xa[2][1] + XOFF + 64); SB;
        const f32x4 xn2_0 = mfma16(CD[2], cb2_0, ZERO4); SB;
        const unsigned xb0_0 = pk2(xn0_2[0], xn0_2[1]); SB;
        const unsigned xb0_2 = pk2(xn1_2[0], xn1_2[1]); SB;
        const unsigned xb0_3 = pk2(xn1_2[2], xn1_2[3]); SB;
        const u32x4 xt0 = u32x4{xb0_0, xb0_1, xb0_2, xb0_3}; XN[0] = xt0;
        const u32x4 cb2_3 = lds128(smem, xa[3][0] + XOFF + 64); SB;
        const u32x4 cb3_3 = lds128(smem, xa[3][1] + XOFF + 64); SB;
        const f32x4 s2_0 = mfma16(xt0, xt0, ZERO4); SB;
        const u32x4 cb2_4 = lds128(smem, xa[4][0] + XOFF + 64); SB;
        const f32x4 xn3_0 = mfma16(CD[3], cb3_0, ZERO4); SB;
        const f32x4 s1_0 = mfma16(ONES, xt0, ZERO4); SB;
        const f32x4 xn2_1 = smf(CS[4], cb2_1, cb2_2, xn2_0, sidx); SB;
        const u32x4 cb3_4 = lds128(smem, xa[4][1] + XOFF + 64); SB;
        const u32x4 cb4_0 = lds128(smem, xa[0][0] + XOFF + 128); SB;
        const f32x4 xn3_1 = smf(CS[6], cb3_1, cb3_2, xn3_0, sidx); SB;
        const u32x4 cb5_0 = lds128(smem, xa[0][1] + XOFF + 128); SB;
        const u32x4 cb4_1 = lds128(smem, xa[1][0] + XOFF + 128); SB;
        const u32x4 cb5_1 = lds128(smem, xa[1][1] + XOFF + 128); SB;
        const u32x4 cb4_2 = lds128(smem, xa[2][0] + XOFF + 128); SB;
        const f32x4 xn2_2 = smf(CS[5], cb2_3, cb2_4, xn2_1, sidx); SB;
        const f32x4 xn3_2 = smf(CS[7], cb3_3, cb3_4, xn3_1, sidx); SB;
        const u32x4 cb5_2 = lds128(smem, xa[2][1] + XOFF + 128); SB;
        const f32x4 xn4_0 = mfma16(CD[4], cb4_0, ZERO4); SB;
        const u32x4 cb4_3 = lds128(smem, xa[3][0] + XOFF + 128); SB;
        const u32x4 cb5_3 = lds128(smem, xa[3][1] + XOFF + 128); SB;
        const unsigned xb1_1 = pk2(xn2_2[2], xn2_2[3]); SB;
        const unsigned xb1_0 = pk2(xn2_2[0], xn2_2[1]); SB;
        const f32x4 xn5_0 = mfma16(CD[5], cb5_0, ZERO4); SB;
        const u32x4 cb4_4 = lds128(smem, xa[4][0] + XOFF + 128); SB;
        const unsigned xb1_2 = pk2(xn3_2[0], xn3_2[1]); SB;
        const unsigned xb1_3 = pk2(xn3_2[2], xn3_2[3]); SB;
        const u32x4 xt1 = u32x4{xb1_0, xb1_1, xb1_2, xb1_3}; XN[1] = xt1;
        const f32x4 s1_1 = mfma16(ONES, xt1, s1_0); SB;
        const f32x4 xn4_1 = smf(CS[8], cb4_1, cb4_2, xn4_0, sidx); SB;
        const u32x4 of2 = lds128(smem, rw + OOFF + 2048); SB;
        const f32x4 xn5_1 = smf(CS[10], cb5_1, cb5_2, xn5_0, sidx); SB;
        const u32x4 cb5_4 = lds128(smem, xa[4][1] + XOFF + 128); SB;
        const f32x4 xn4_2 = smf(CS[9], cb4_3, cb4_4, xn4_1, sidx); SB;
        const u32x4 wo0_0 = lds128(smem, wob + 0); SB;
        const f32x4 s2_1 = mfma16(xt1, xt1, s2_0); SB;
        const u32x4 of1 = lds128(smem, rw + OOFF + 1024); SB;
        const u32x4 of0 = lds128(smem, rw + OOFF + 0); SB;
        const u32x4 wo0_1 = lds128(smem, wob + 1024); SB;
        const unsigned xb2_0 = pk2(xn4_2[0], xn4_2[1]); SB;
        const unsigned xb2_1 = pk2(xn4_2[2], xn4_2[3]); SB;
        const f32x4 xn5_2 = smf(CS[11], cb5_3, cb5_4, xn5_1, sidx); SB;
        const u32x4 wo0_2 = lds128(smem, wob + 2048); SB;
        const u32x4 wo1_0 = lds128(smem, wob + 3072); SB;
        const f32x4 p0_0 = mfma16(wo0_0, of0, pi0); SB;
        const u32x4 wo1_1 = lds128(smem, wob + 4096); SB;
        const f32x4 p0_1 = mfma16(wo0_1, of1, p0_0); SB;
        const u32x4 wo1_2 = lds128(smem, wob + 5120); SB;
        const f32x4 p0_2 = mfma16(wo0_2, of2, p0_1); SB;
        const unsigned xb2_2 = pk2(xn5_2[0], xn5_2[1]); SB;
        const unsigned xb2_3 = pk2(xn5_2[2], xn5_2[3]); SB;
        const u32x4 xt2 = u32x4{xb2_0, xb2_1, xb2_2, xb2_3}; XN[2] = xt2;
        const f32x4 s2_2 = mfma16(xt2, xt2, s2_1); SB;
        const f32x4 s1_2 = mfma16(ONES, xt2, s1_1); SB;
        const u32x4 wo2_0 = lds128(smem, wob + 6144); SB;
        const f32x4 p1_0 = mfma16(wo1_0, of0, pi1); SB;
        const u32x4 wo2_1 = lds128(smem, wob + 7168); SB;
        const f32x4 p1_1 = mfma16(wo1_1, of1, p1_0); SB;
        const u32x4 wo2_2 = lds128(smem, wob + 8192); SB;
        *(u32x4*)(smem + rq + QOFF) = XC[2]; SB;
        const f32x4 p1_2 = mfma16(wo1_2, of2, p1_1); SB;
        const float d0 = dm0 ? s2_2[0] : 0.0f; SB;
        const float d1 = dm1 ? s2_2[1] : d0; SB;
        const float d2 = dm2 ? s2_2[2] : d1; SB;
        const float d3 = dm3 ? s2_2[3] : d2; SB;
        const float mean = s1_2[0] * (1.0f / 96.0f); SB;
        const u32x4 wo3_0 = lds128(smem, wob + 9216); SB;
        const float tv = fmaf(-mean, mean, 1e-5f); SB;
        const u32x4 wo3_1 = lds128(smem, wob + 10240); SB;
        const u32x4 wo3_2 = lds128(smem, wob + 11264); SB;
        const unsigned po0_0 = pk2(p0_2[0], p0_2[1]); SB;
        const unsigned po0_1 = pk2(p0_2[2], p0_2[3]); SB;
        const f32x4 p2_0 = mfma16(wo2_0, of0, pi2); SB;
        const unsigned po1_0 = pk2(p1_2[0], p1_2[1]); SB;
        const unsigned po1_1 = pk2(p1_2[2], p1_2[3]); SB;
        __builtin_amdgcn_raw_buffer_store_b128(u32x4{po0_0, po0_1, po1_0, po1_1}, psrd, orel, obase + 0, ST_AUX); SB;
        const f32x4 sq = __builtin_amdgcn_mfma_f32_16x16x4f32(1.0f, d3, ZERO4, 0, 0, 0); SB;
        const f32x4 p3_0 = mfma16(wo3_0, of0, pi3); SB;
        const float var = fmaf(sq[0], 1.0f / 96.0f, tv); SB;
        const float varc = __builtin_amdgcn_fmed3f(var, 1e-5f, 3.0e38f); SB;
        const f32x4 p2_1 = mfma16(wo2_1, of1, p2_0); SB;
        const float rstd = __builtin_amdgcn_rsqf(varc); SB;
        const f32x4 p2_2 = mfma16(wo2_2, of2, p2_1); SB;
        __builtin_amdgcn_s_setprio(0); SB;
        const float nm = -mean * rstd; SB;
        const float h0_0_0 = fmaf(xn0_2[0], rstd, nm); SB;
        const float h0_1_0 = fmaf(xn0_2[2], rstd, nm); SB;
        const float h0_0_1 = fmaf(xn0_2[1], rstd, nm); SB;
        const float h0_1_1 = fmaf(xn0_2[3], rstd, nm); SB;
        const float h0_2_0 = fmaf(xn1_2[0], rstd, nm); SB;
        const float h0_2_1 = fmaf(xn1_2[1], rstd, nm); SB;
        const float h0_3_0 = fmaf(xn1_2[2], rstd, nm); SB;
        const float h1_2_0 = fmaf(xn3_2[0], rstd, nm); SB;
        const float h1_2_1 = fmaf(xn3_2[1], rstd, nm); SB;
        const float h1_3_0 = fmaf(xn3_2[2], rstd, nm); SB;
        const float h1_3_1 = fmaf(xn3_2[3], rstd, nm); SB;
        const float h2_0_0 = fmaf(xn4_2[0], rstd, nm); SB;
        const float h2_0_1 = fmaf(xn4_2[1], rstd, nm); SB;
        const float h0_3_1 = fmaf(xn1_2[3], rstd, nm); SB;
        const float h1_0_0 = fmaf(xn2_2[0], rstd, nm); SB;
        const float h1_0_1 = fmaf(xn2_2[1], rstd, nm); SB;
        const float h1_1_0 = fmaf(xn2_2[2], rstd, nm); SB;
        const float h1_1_1 = fmaf(xn2_2[3], rstd, nm); SB;
        const float h2_1_0 = fmaf(xn4_2[2], rstd, nm); SB;
        const float h2_1_1 = fmaf(xn4_2[3], rstd, nm); SB;
        const float h2_2_0 = fmaf(xn5_2[0], rstd, nm); SB;
        const float h2_2_1 = fmaf(xn5_2[1], rstd, nm); SB;
        const unsigned hv1_0 = pk2(h1_0_0, h1_0_1); SB;
        const unsigned hv1_1 = pk2(h1_1_0, h1_1_1); SB;
        const unsigned hv1_2 = pk2(h1_2_0, h1_2_1); SB;
        const unsigned hv1_3 = pk2(h1_3_0, h1_3_1); SB;
        const unsigned hv2_0 = pk2(h2_0_0, h2_0_1); SB;
        const unsigned hv2_1 = pk2(h2_1_0, h2_1_1); SB;
        const float h2_3_0 = fmaf(xn5_2[2], rstd, nm); SB;
        const float h2_3_1 = fmaf(xn5_2[3], rstd, nm); SB;
        const unsigned hv0_0 = pk2(h0_0_0, h0_0_1); SB;
        const unsigned hv0_1 = pk2(h0_1_0, h0_1_1); SB;
        const unsigned hv0_2 = pk2(h0_2_0, h0_2_1); SB;
        const unsigned hv0_3 = pk2(h0_3_0, h0_3_1); SB;
        *(u32x4*)(smem + rw + HOFF + 0) = u32x4{hv0_0, hv0_1, hv0_2, hv0_3}; SB;
        const unsigned po2_0 = pk2(p2_2[0], p2_2[1]); SB;
        *(u32x4*)(smem + rw + HOFF + 1024) = u32x4{hv1_0, hv1_1, hv1_2, hv1_3}; SB;
        const unsigned hv2_3 = pk2(h2_3_0, h2_3_1); SB;
        const unsigned hv2_2 = pk2(h2_2_0, h2_2_1); SB;
        *(u32x4*)(smem + rw + HOFF + 2048) = u32x4{hv2_0, hv2_1, hv2_2, hv2_3}; SB;
        const unsigned po2_1 = pk2(p2_2[2], p2_2[3]); SB;
        const f32x4 p3_1 = mfma16(wo3_1, of1, p3_0); SB;
        const f32x4 p3_2 = mfma16(wo3_2, of2, p3_1); SB;
        const unsigned po3_0 = pk2(p3_2[0], p3_2[1]); SB;
        const unsigned po3_1 = pk2(p3_2[2], p3_2[3]); SB;
        __builtin_amdgcn_raw_buffer_store_b128(u32x4{po2_0, po2_1, po3_0, po3_1}, psrd, orel, obase + 64, ST_AUX); SB;
        } else {
        __builtin_amdgcn_s_setprio(2); SB;
        const f32x4 pi0 = mfma16(SEL0, XP[0], ZERO4); SB;
        const u32x4 cb0_0 = lds128(smem, xa[0][0] + XOFF + 0); SB;
        const u32x4 cb1_0 = lds128(smem, xa[0][1] + XOFF + 0); SB;
        const f32x4 pi1 = mfma16(SEL1, XP[0], ZERO4); SB;
        const u32x4 cb0_1 = lds128(smem, xa[1][0] + XOFF + 0); SB;
        const u32x4 cb1_1 = lds128(smem, xa[1][1] + XOFF + 0); SB;
        const u32x4 cb0_2 = lds128(smem, xa[2][0] + XOFF + 0); SB;
        const f32x4 pi2 = mfma16(SEL0, XP[1], ZERO4); SB;
        const u32x4 cb1_2 = lds128(smem, xa[2][1] + XOFF + 0); SB;
        const u32x4 cb0_3 = lds128(smem, xa[3][0] + XOFF + 0); SB;
        const u32x4 cb1_3 = lds128(smem, xa[3][1] + XOFF + 0); SB;
        const f32x4 xn0_0 = mfma16(CD[0], cb0_0, ZERO4); SB;
        const f32x4 xn1_0 = mfma16(CD[1], cb1_0, ZERO4); SB;
        const u32x4 cb0_4 = lds128(smem, xa[4][0] + XOFF + 0); SB;
        const f32x4 pi3 = mfma16(SEL1, XP[1], ZERO4); SB;
        const u32x4 cb1_4 = lds128(smem, xa[4][1] + XOFF + 0); SB;
        const f32x4 xn0_1 = smf(CS[0], cb0_1, cb0_2, xn0_0, sidx); SB;
        const f32x4 xn1_1 = smf(CS[2], cb1_1, cb1_2, xn1_0, sidx); SB;
        const u32x4 cb2_0 = lds128(smem, xa[0][0] + XOFF + 64); SB;
        const u32x4 cb3_0 = lds128(smem, xa[0][1] + XOFF + 64); SB;
        const f32x4 xn0_2 = smf(CS[1], cb0_3, cb0_4, xn0_1, sidx); SB;
        const u32x4 cb2_1 = lds128(smem, xa[1][0] + XOFF + 64); SB;
        const u32x4 cb3_1 = lds128(smem, xa[1][1] + XOFF + 64); SB;
        const f32x4 xn1_2 = smf(CS[3], cb1_3, cb1_4, xn1_1, sidx); SB;
        const u32x4 cb2_2 = lds128(smem, xa[2][0] + XOFF + 64); SB;
        const u32x4 cb3_2 = lds128(smem, xa[2][1] + XOFF + 64); SB;
        const f32x4 xn2_0 = mfma16(CD[2], cb2_0, ZERO4); SB;
        const unsigned xb0_0 = pk2(xn0_2[0], xn0_2[1]); SB;
        const unsigned xb0_1 = pk2(xn0_2[2], xn0_2[3]); SB;
        const unsigned xb0_2 = pk2(xn1_2[0], xn1_2[1]); SB;
        const unsigned xb0_3 = pk2(xn1_2[2], xn1_2[3]); SB;
        const u32x4 xt0 = u32x4{xb0_0, xb0_1, xb0_2, xb0_3}; XN[0] = xt0;
        const u32x4 cb2_3 = lds128(smem, xa[3][0] + XOFF + 64); SB;
        const u32x4 cb3_3 = lds128(smem, xa[3][1] + XOFF + 64); SB;
        const f32x4 s2_0 = mfma16(xt0, xt0, ZERO4); SB;
        const u32x4 cb2_4 = lds128(smem, xa[4][0] + XOFF + 64); SB;
        const f32x4 xn3_0 = mfma16(CD[3], cb3_0, ZERO4); SB;
        const f32x4 s1_0 = mfma16(ONES, xt0, ZERO4); SB;
        const u32x4 cb3_4 = lds128(smem, xa[4][1] + XOFF + 64); SB;
        const u32x4 cb4_0 = lds128(smem, xa[0][0] + XOFF + 128); SB;
        const f32x4 xn2_1 = smf(CS[4], cb2_1, cb2_2, xn2_0, sidx); SB;
        const f32x4 xn3_1 = smf(CS[6], cb3_1, cb3_2, xn3_0, sidx); SB;
        const u32x4 cb5_0 = lds128(smem, xa[0][1] + XOFF + 128); SB;
        const u32x4 cb4_1 = lds128(smem, xa[1][0] + XOFF + 128); SB;
        const u32x4 cb5_1 = lds128(smem, xa[1][1] + XOFF + 128); SB;
        const u32x4 cb4_2 = lds128(smem, xa[2][0] + XOFF + 128); SB;
        const f32x4 xn2_2 = smf(CS[5], cb2_3, cb2_4, xn2_1, sidx); SB;
        const f32x4 xn3_2 = smf(CS[7], cb3_3, cb3_4, xn3_1, sidx); SB;
        const u32x4 cb5_2 = lds128(smem, xa[2][1] + XOFF + 128); SB;
        const u32x4 cb4_3 = lds128(smem, xa[3][0] + XOFF + 128); SB;
        const u32x4 cb5_3 = lds128(smem, xa[3][1] + XOFF + 128); SB;
        const f32x4 xn4_0 = mfma16(CD[4], cb4_0, ZERO4); SB;
        const unsigned xb1_0 = pk2(xn2_2[0], xn2_2[1]); SB;
        const unsigned xb1_1 = pk2(xn2_2[2], xn2_2[3]); SB;
        const f32x4 xn5_0 = mfma16(CD[5], cb5_0, ZERO4); SB;
        const u32x4 cb4_4 = lds128(smem, xa[4][0] + XOFF + 128); SB;
        const unsigned xb1_2 = pk2(xn3_2[0], xn3_2[1]); SB;
        const unsigned xb1_3 = pk2(xn3_2[2], xn3_2[3]); SB;
        const u32x4 xt1 = u32x4{xb1_0, xb1_1, xb1_2, xb1_3}; XN[1] = xt1;
        const f32x4 s1_1 = mfma16(ONES, xt1, s1_0); SB;
        const f32x4 xn4_1 = smf(CS[8], cb4_1, cb4_2, xn4_0, sidx); SB;
        const u32x4 of2 = lds128(smem, rw + OOFF + 2048); SB;
        const f32x4 xn5_1 = smf(CS[10], cb5_1, cb5_2, xn5_0, sidx); SB;
        const u32x4 cb5_4 = lds128(smem, xa[4][1] + XOFF + 128); SB;
        const f32x4 xn4_2 = smf(CS[9], cb4_3, cb4_4, xn4_1, sidx); SB;
        const u32x4 wo0_0 = lds128(smem, wob + 0); SB;
        const f32x4 s2_1 = mfma16(xt1, xt1, s2_0); SB;
        const u32x4 of1 = lds128(smem, rw + OOFF + 1024); SB;
        const u32x4 of0 = lds128(smem, rw + OOFF + 0); SB;
        const u32x4 wo0_1 = lds128(smem, wob + 1024); SB;
        const unsigned xb2_0 = pk2(xn4_2[0], xn4_2[1]); SB;
        const unsigned xb2_1 = pk2(xn4_2[2], xn4_2[3]); SB;
        const f32x4 xn5_2 = smf(CS[11], cb5_3, cb5_4, xn5_1, sidx); SB;
        const u32x4 wo0_2 = lds128(smem, wob + 2048); SB;
        const u32x4 wo1_0 = lds128(smem, wob + 3072); SB;
        const f32x4 p0_0 = mfma16(wo0_0, of0, pi0); SB;
        const u32x4 wo1_1 = lds128(smem, wob + 4096); SB;
        const f32x4 p0_1 = mfma16(wo0_1, of1, p0_0); SB;
        const u32x4 wo1_2 = lds128(smem, wob + 5120); SB;
        const f32x4 p0_2 = mfma16(wo0_2, of2, p0_1); SB;
        const unsigned xb2_2 = pk2(xn5_2[0], xn5_2[1]); SB;
        const unsigned xb2_3 = pk2(xn5_2[2], xn5_2[3]); SB;
        const u32x4 xt2 = u32x4{xb2_0, xb2_1, xb2_2, xb2_3}; XN[2] = xt2;
        const f32x4 s2_2 = mfma16(xt2, xt2, s2_1); SB;
        const f32x4 s1_2 = mfma16(ONES, xt2, s1_1); SB;
        const u32x4 wo2_0 = lds128(smem, wob + 6144); SB;
        const f32x4 p1_0 = mfma16(wo1_0, of0, pi1); SB;
        const u32x4 wo2_1 = lds128(smem, wob + 7168); SB;
        const f32x4 p1_1 = mfma16(wo1_1, of1, p1_0); SB;
        const float d0 = dm0 ? s2_2[0] : 0.0f; SB;
        const float d1 = dm1 ? s2_2[1] : d0; SB;
        const float d2 = dm2 ? s2_2[2] : d1; SB;
        const float d3 = dm3 ? s2_2[3] : d2; SB;
        const u32x4 wo2_2 = lds128(smem, wob + 8192); SB;
        *(u32x4*)(smem + rq + QOFF) = XC[2]; SB;
        const f32x4 p1_2 = mfma16(wo1_2, of2, p1_1); SB;
        const float mean = s1_2[0] * (1.0f / 96.0f); SB;
        const u32x4 wo3_0 = lds128(smem, wob + 9216); SB;
        const float tv = fmaf(-mean, mean, 1e-5f); SB;
        const u32x4 wo3_1 = lds128(smem, wob + 10240); SB;
        const u32x4 wo3_2 = lds128(smem, wob + 11264); SB;
        const unsigned po0_0 = pk2(p0_2[0], p0_2[1]); SB;
        const unsigned po0_1 = pk2(p0_2[2], p0_2[3]); SB;
        const f32x4 p2_0 = mfma16(wo2_0, of0, pi2); SB;
        const unsigned po1_0 = pk2(p1_2[0], p1_2[1]); SB;
        const unsigned po1_1 = pk2(p1_2[2], p1_2[3]); SB;
        __builtin_amdgcn_raw_buffer_store_b128(u32x4{po0_0, po0_1, po1_0, po1_1}, psrd, orel, obase + 0, ST_AUX); SB;
        const f32x4 sq = __builtin_amdgcn_mfma_f32_16x16x4f32(1.0f, d3, ZERO4, 0, 0, 0); SB;
        const float var = fmaf(sq[0], 1.0f / 96.0f, tv); SB;
        const f32x4 p3_0 = mfma16(wo3_0, of0, pi3); SB;
        __builtin_amdgcn_s_setprio(0); SB;
        const float varc = __builtin_amdgcn_fmed3f(var, 1e-5f, 3.0e38f); SB;
        const f32x4 p2_1 = mfma16(wo2_1, of1, p2_0); SB;
        const float rstd = __builtin_amdgcn_rsqf(varc); SB;
        const f32x4 p2_2 = mfma16(wo2_2, of2, p2_1); SB;
        const float nm = -mean * rstd; SB;
        const float h0_0_0 = fmaf(xn0_2[0], rstd, nm); SB;
        const float h0_0_1 = fmaf(xn0_2[1], rstd, nm); SB;
        const float h0_1_0 = fmaf(xn0_2[2], rstd, nm); SB;
        const float h0_1_1 = fmaf(xn0_2[3], rstd, nm); SB;
        const float h0_2_0 = fmaf(xn1_2[0], rstd, nm); SB;
        const float h0_2_1 = fmaf(xn1_2[1], rstd, nm); SB;
        const float h0_3_0 = fmaf(xn1_2[2], rstd, nm); SB;
        const float h1_3_0 = fmaf(xn3_2[2], rstd, nm); SB;
        const float h1_3_1 = fmaf(xn3_2[3], rstd, nm); SB;
        const float h1_2_0 = fmaf(xn3_2[0], rstd, nm); SB;
        const float h1_2_1 = fmaf(xn3_2[1], rstd, nm); SB;
        const float h2_0_0 = fmaf(xn4_2[0], rstd, nm); SB;
        const float h2_0_1 = fmaf(xn4_2[1], rstd, nm); SB;
        const float h0_3_1 = fmaf(xn1_2[3], rstd, nm); SB;
        const float h1_0_0 = fmaf(xn2_2[0], rstd, nm); SB;
        const float h1_0_1 = fmaf(xn2_2[1], rstd, nm); SB;
        const float h1_1_0 = fmaf(xn2_2[2], rstd, nm); SB;
        const float h1_1_1 = fmaf(xn2_2[3], rstd, nm); SB;
        const float h2_1_0 = fmaf(xn4_2[2], rstd, nm); SB;
        const float h2_1_1 = fmaf(xn4_2[3], rstd, nm); SB;
        const float h2_2_0 = fmaf(xn5_2[0], rstd, nm); SB;
        const float h2_3_0 = fmaf(xn5_2[2], rstd, nm); SB;
        const float h2_3_1 = fmaf(xn5_2[3], rstd, nm); SB;
        const unsigned hv0_0 = pk2(h0_0_0, h0_0_1); SB;
        const unsigned hv0_1 = pk2(h0_1_0, h0_1_1); SB;
        const unsigned hv0_3 = pk2(h0_3_0, h0_3_1); SB;
        const unsigned hv1_0 = pk2(h1_0_0, h1_0_1); SB;
        const unsigned hv1_1 = pk2(h1_1_0, h1_1_1); SB;
        const float h2_2_1 = fmaf(xn5_2[1], rstd, nm); SB;
        const unsigned hv0_2 = pk2(h0_2_0, h0_2_1); SB;
        const unsigned hv1_2 = pk2(h1_2_0, h1_2_1); SB;
        const unsigned hv1_3 = pk2(h1_3_0, h1_3_1); SB;
        const unsigned hv2_0 = pk2(h2_0_0, h2_0_1); SB;
        *(u32x4*)(smem + rw + HOFF + 0) = u32x4{hv0_0, hv0_1, hv0_2, hv0_3}; SB;
        const unsigned hv2_1 = pk2(h2_1_0, h2_1_1); SB;
        const unsigned hv2_3 = pk2(h2_3_0, h2_3_1); SB;
        const unsigned hv2_2 = pk2(h2_2_0, h2_2_1); SB;
        const unsigned po2_0 = pk2(p2_2[0], p2_2[1]); SB;
        *(u32x4*)(smem + rw + HOFF + 2048) = u32x4{hv2_0, hv2_1, hv2_2, hv2_3}; SB;
        *(u32x4*)(smem + rw + HOFF + 1024) = u32x4{hv1_0, hv1_1, hv1_2, hv1_3}; SB;
        const unsigned po2_1 = pk2(p2_2[2], p2_2[3]); SB;
        const f32x4 p3_1 = mfma16(wo3_1, of1, p3_0); SB;
        const f32x4 p3_2 = mfma16(wo3_2, of2, p3_1); SB;
        const unsigned po3_0 = pk2(p3_2[0], p3_2[1]); SB;
        const unsigned po3_1 = pk2(p3_2[2], p3_2[3]); SB;
        __builtin_amdgcn_raw_buffer_store_b128(u32x4{po2_0, po2_1, po3_0, po3_1}, psrd, orel, obase + 64, ST_AUX); SB;
        }
        if constexpr (V == 0) {
          if (n == PF2_STEP) {
            u32x4 csrd;
            csrd[0] = (unsigned)pc & ~255u; csrd[1] = (unsigned)(pc >> 32) & 0xffffu; csrd[2] = CODE_BYTES; csrd[3] = 0x00020000u;
            dma16<XB + 15360>(ldsbase, l16 + (unsigned)(idx * 3 + w) * 1024u, csrd, 0u);
          }
        }
        obase += ocx >= NWS - PERX ? STEP_WRAP : STEP_IN;
        ocx += ocx >= NWS - PERX ? PERX - NWS : PERX;
        asm volatile("" : "+s"(obase), "+s"(ocx));
        if constexpr (DDM != 0) cur_next(ta);
        STAMP(20 + V);
        if constexpr ((DDM >> ((V + 3) & 3)) & 1) bar_vm<(((DDM >> V) & 1) ? 4 : 0) + 2>();
        else BAR_LGKM();
        STAMP(24 + V);
      };
#pragma nounroll
      for (int n = 0; n < 20; n += 4) {
        stepA(IC<0>{}, n); stepA(IC<1>{}, n + 1);
        if (n + 4 < nwin) stepA(IC<2>{}, n + 2);
        if (n + 5 < nwin) stepA(IC<3>{}, n + 3);
      }
      {
        __builtin_amdgcn_s_setprio(TPA);
        const __amdgpu_buffer_rsrc_t psrd = osrd;
        u32x4 WOt[12];
#pragma unroll
        for (int k = 0; k < 12; ++k) WOt[k] = lds128(smem, wob + k * 1024);
        const unsigned xfree = xfree_of(nwin);
        auto leftA = [&](auto NC, auto WC) {
          constexpr int N = decltype(NC)::value;
          const u32x4* WP = WOt + 6 * decltype(WC)::value;
          const unsigned rolA = OL + (N - 20) * 3072 + l16, xlA = xfree + (N - 20) * 2048 + decltype(WC)::value * 1024 + l16;
          const unsigned oLA = *(const unsigned*)(smem + xfree + 4096 + (N - 20) * 256 + lane * 4);
          SB;
        const u32x4 ol0 = lds128(smem, rolA + 0); SB;
        const u32x4 ol1 = lds128(smem, rolA + 1024); SB;
        const u32x4 ol2 = lds128(smem, rolA + 2048); SB;
        const u32x4 xr = lds128(smem, xlA); SB;
        const f32x4 pia0 = mfma16(SEL0, xr, ZERO4); SB;
        const f32x4 pia1 = mfma16(SEL1, xr, ZERO4); SB;
        const f32x4 pa0_0 = mfma16(WP[0], ol0, pia0); SB;
        const f32x4 pa1_0 = mfma16(WP[3], ol0, pia1); SB;
        const f32x4 pa0_1 = mfma16(WP[1], ol1, pa0_0); SB;
        const f32x4 pa1_1 = mfma16(WP[4], ol1, pa1_0); SB;
        const f32x4 pa0_2 = mfma16(WP[2], ol2, pa0_1); SB;
        const f32x4 pa1_2 = mfma16(WP[5], ol2, pa1_1); SB;
        const unsigned poa0_0 = pk2(pa0_2[0], pa0_2[1]); SB;
        const unsigned poa0_1 = pk2(pa0_2[2], pa0_2[3]); SB;
        const unsigned poa1_0 = pk2(pa1_2[0], pa1_2[1]); SB;
        const unsigned poa1_1 = pk2(pa1_2[2], pa1_2[3]); SB;
        __builtin_amdgcn_raw_buffer_store_b128(u32x4{poa0_0, poa0_1, poa1_0, poa1_1}, osrd, oLA, w * 64, ST_AUX); SB;
        };
        auto tailA = [&](auto NC) {
          constexpr int N = decltype(NC)::value;
          constexpr int V = N & 3;
          constexpr int OOFF = OR_ - H1 + ((N + 1) & 1) * 9216, QOFF = (N & 1) * 3072;
          u32x4* XP = V == 0 ? XS3 : (V == 1 ? XS0 : (V == 2 ? XS1 : XS2));
          u32x4* XC = V == 0 ? XS0 : (V == 1 ? XS1 : (V == 2 ? XS2 : XS3));
          SB;
        *(u32x4*)(smem + rq + QOFF) = XC[2]; SB;
        const u32x4 of0 = lds128(smem, rw + OOFF + 0); SB;
        const u32x4 of1 = lds128(smem, rw + OOFF + 1024); SB;
        const u32x4 of2 = lds128(smem, rw + OOFF + 2048); SB;
        const f32x4 pi0 = mfma16(SEL0, XP[0], ZERO4); SB;
        const f32x4 pi1 = mfma16(SEL1, XP[0], ZERO4); SB;
        const f32x4 p0_0 = mfma16(WOt[0], of0, pi0); SB;
        const f32x4 p1_0 = mfma16(WOt[3], of0, pi1); SB;
        const f32x4 p0_1 = mfma16(WOt[1], of1, p0_0); SB;
        const f32x4 p1_1 = mfma16(WOt[4], of1, p1_0); SB;
        const f32x4 p0_2 = mfma16(WOt[2], of2, p0_1); SB;
        const f32x4 p1_2 = mfma16(WOt[5], of2, p1_1); SB;
        const f32x4 pi2 = mfma16(SEL0, XP[1], ZERO4); SB;
        const f32x4 pi3 = mfma16(SEL1, XP[1], ZERO4); SB;
        const unsigned po0_0 = pk2(p0_2[0], p0_2[1]); SB;
        const f32x4 p2_0 = mfma16(WOt[6], of0, pi2); SB;
        const unsigned po0_1 = pk2(p0_2[2], p0_2[3]); SB;
        const f32x4 p3_0 = mfma16(WOt[9], of0, pi3); SB;
        const unsigned po1_0 = pk2(p1_2[0], p1_2[1]); SB;
        const f32x4 p2_1 = mfma16(WOt[7], of1, p2_0); SB;
        const unsigned po1_1 = pk2(p1_2[2], p1_2[3]); SB;
        const f32x4 p3_1 = mfma16(WOt[10], of1, p3_0); SB;
        __builtin_amdgcn_raw_buffer_store_b128(u32x4{po0_0, po0_1, po1_0, po1_1}, psrd, orel, obase + 0, ST_AUX); SB;
        const f32x4 p2_2 = mfma16(WOt[8], of2, p2_1); SB;
        const f32x4 p3_2 = mfma16(WOt[11], of2, p3_1); SB;
        const unsigned po2_0 = pk2(p2_2[0], p2_2[1]); SB;
        const unsigned po2_1 = pk2(p2_2[2], p2_2[3]); SB;
        const unsigned po3_0 = pk2(p3_2[0], p3_2[1]); SB;
        const unsigned po3_1 = pk2(p3_2[2], p3_2[3]); SB;
        __builtin_amdgcn_raw_buffer_store_b128(u32x4{po2_0, po2_1, po3_0, po3_1}, psrd, orel, obase + 64, ST_AUX); SB;
          if constexpr (N >= 20) {
            if (w == 0) leftA(NC, IC<0>{}); else if (w == 1) leftA(NC, IC<1>{});
          }
        };
        auto endA = [&]() {
          obase += ocx >= NWS - PERX ? STEP_WRAP : STEP_IN;
          ocx += ocx >= NWS - PERX ? PERX - NWS : PERX;
          asm volatile("" : "+s"(obase), "+s"(ocx));
          BAR_LGKM();
        };
        if (!(nwin & 1)) { tailA(IC<18>{}); endA(); }
        tailA(IC<19>{}); endA();
        tailA(IC<20>{});
        if (nwin & 1) { endA(); tailA(IC<21>{}); }
      }
      TSLOUT();
      STAMP(10);
    } else {
      unsigned rel[15];
      unsigned xl[5], orel[3], orelL = OOB;
      {
        const int hf = g >> 1, e = g & 1;
        const int d0[5] = {0, 1, 2, 4, 6};
        const int d1[5] = {3, 4, 5, 7, 8};
#pragma unroll
        for (int s = 0; s < 5; ++s) xl[s] = XL + (t & 3) * XLS + (hf ? d1[s] : d0[s]) * 192 + e * 16;
#pragma unroll
        for (int T = 0; T < 3; ++T) {
          int ty, tx;
          tok(T, ty, tx);
          orel[T] = (ty * IMG + tx) * PIXB + g * 16;
        }
      }
      unsigned ro = OR_ + l16, rq = XQ + l16, wob = WO + l16, rol = OL + 3072 + l16, hlw = HL0 + l16;
      asm volatile("" : "+v"(ro), "+v"(rq), "+v"(wob), "+v"(rol), "+v"(hlw));
      u32x4 XLc[3], XLn[3], W45[6];
      f32x4 xp[6];
#pragma unroll
      for (int k = 0; k < 3; ++k) XLc[k] = u32x4{0u, 0u, 0u, 0u};
      int dci = 3;
      Cur tc = cur_at(3);
      Cur xc = cur_at(8);
      int xm = 8;
      auto dma_gen = [&](auto SC, auto Q0C, auto NQC, unsigned tdst, const Cur& tc, int dci) {
        constexpr int SLOT = decltype(SC)::value, Q0 = decltype(Q0C)::value, NQ = decltype(NQC)::value;
        const unsigned inb = tc.base - (unsigned)((IMG + 1) * PIXB);
        u32x4 srd = xsrd;
        srd[2] = dci < nwin ? XBYTES : 0u;
        const bool lr = (tc.cx == 0) | (tc.cx == NWS - 1);
        const bool tb = (tc.cy == 0) | (tc.cy == NWS - 1);
        auto all = [&](auto&& f) {
          auto one = [&](auto QC) { if constexpr (decltype(QC)::value >= Q0 && decltype(QC)::value < Q0 + NQ) f(QC); };
          one(IC<0>{}); one(IC<1>{}); one(IC<2>{}); one(IC<3>{}); one(IC<4>{}); one(IC<5>{}); one(IC<6>{}); one(IC<7>{});
          one(IC<8>{}); one(IC<9>{}); one(IC<10>{}); one(IC<11>{}); one(IC<12>{}); one(IC<13>{}); one(IC<14>{});
        };
        if (__builtin_expect(lr, 0)) {
          const unsigned ln = lane_id_opaque();
          all([&](auto QC) {
            constexpr int q = decltype(QC)::value;
            dma16x<XB + SLOT * 16384 + q * 1024>(tdst, (rel[q] + inb) | tile_cut(tile_src(q * 64 + ln, q == 13), tc.cx == 0, tc.cx == NWS - 1), srd, 0u);
          });
        } else if (__builtin_expect(tb, 0)) {
          all([&](auto QC) {
            constexpr int q = decltype(QC)::value;
            dma16x<XB + SLOT * 16384 + q * 1024>(tdst, rel[q] + inb, srd, 0u);
          });
        } else {
          all([&](auto QC) {
            constexpr int q = decltype(QC)::value;
            dma16x<XB + SLOT * 16384 + q * 1024>(tdst, rel[q], srd, inb);
          });
        }
      };
      auto dma_part = [&](auto SC, auto Q0C, auto NQC, unsigned tdst) { dma_gen(SC, Q0C, NQC, tdst, tc, dci); };
      auto dma_next = [&](auto SC, auto Q0C, auto NQC, unsigned tdst) {
        Cur tn = tc;
        cur_next(tn);
        dma_gen(SC, Q0C, NQC, tdst, tn, dci + 1);
      };
      auto dma_done = [&]() { cur_next(tc); ++dci; };
      auto dma_tile = [&](auto SC, unsigned tdst) { dma_part(SC, IC<0>{}, IC<15>{}, tdst); dma_done(); };
      auto xl_next = [&](int j) { xl_one(ldsbase + j * XLS, xc, xm < nwin); cur_next(xc); ++xm; };
      WAIT_VM(6);
      TS(3);
      STAMP(8);
      build_conv();
      STAMP(9);
      TS(4);
      first_tile_landed();
      TS(5);
      STAMP(0);
      {
        const u32x4 cb0_0 = lds128(smem, xl[0] + 0); SB;
        const u32x4 cb1_0 = lds128(smem, xl[0] + 32); SB;
        const u32x4 cb0_1 = lds128(smem, xl[1] + 0); SB;
        const u32x4 cb1_1 = lds128(smem, xl[1] + 32); SB;
        const u32x4 cb0_2 = lds128(smem, xl[2] + 0); SB;
        const u32x4 cb1_2 = lds128(smem, xl[2] + 32); SB;
        const u32x4 cb0_3 = lds128(smem, xl[3] + 0); SB;
        const u32x4 cb1_3 = lds128(smem, xl[3] + 32); SB;
        const f32x4 xn0_0 = mfma16(CD[0], cb0_0, ZERO4); SB;
        const u32x4 cb0_4 = lds128(smem, xl[4] + 0); SB;
        const f32x4 xn1_0 = mfma16(CD[1], cb1_0, ZERO4); SB;
        const u32x4 cb1_4 = lds128(smem, xl[4] + 32); SB;
        const u32x4 cb2_0 = lds128(smem, xl[0] + 64); SB;
        const u32x4 cb3_0 = lds128(smem, xl[0] + 96); SB;
        const f32x4 xn0_1 = smf(CS[0], cb0_1, cb0_2, xn0_0, sidx); SB;
        const u32x4 cb2_1 = lds128(smem, xl[1] + 64); SB;
        const f32x4 xn1_1 = smf(CS[2], cb1_1, cb1_2, xn1_0, sidx); SB;
        const u32x4 cb3_1 = lds128(smem, xl[1] + 96); SB;
        const u32x4 cb2_2 = lds128(smem, xl[2] + 64); SB;
        const u32x4 cb3_2 = lds128(smem, xl[2] + 96); SB;
        const f32x4 xn0_2 = smf(CS[1], cb0_3, cb0_4, xn0_1, sidx); SB;
        const u32x4 cb2_3 = lds128(smem, xl[3] + 64); SB;
        const f32x4 xn1_2 = smf(CS[3], cb1_3, cb1_4, xn1_1, sidx); SB;
        const u32x4 cb3_3 = lds128(smem, xl[3] + 96); SB;
        const f32x4 xn2_0 = mfma16(CD[2], cb2_0, ZERO4); SB;
        const u32x4 cb2_4 = lds128(smem, xl[4] + 64); SB;
        const f32x4 xn3_0 = mfma16(CD[3], cb3_0, ZERO4); SB;
        const u32x4 cb3_4 = lds128(smem, xl[4] + 96); SB;
        const unsigned xb0_0 = pk2(xn0_2[0], xn0_2[1]); SB;
        const unsigned xb0_1 = pk2(xn0_2[2], xn0_2[3]); SB;
        const unsigned xb0_2 = pk2(xn1_2[0], xn1_2[1]); SB;
        const unsigned xb0_3 = pk2(xn1_2[2], xn1_2[3]); SB;
        const u32x4 xt0 = u32x4{xb0_0, xb0_1, xb0_2, xb0_3}; XLn[0] = xt0;
        const f32x4 s2_0 = mfma16(xt0, xt0, ZERO4); SB;
        const u32x4 cb4_0 = lds128(smem, xl[0] + 128); SB;
        const f32x4 xn2_1 = smf(CS[4], cb2_1, cb2_2, xn2_0, sidx); SB;
        const u32x4 cb5_0 = lds128(smem, xl[0] + 160); SB;
        const u32x4 cb4_1 = lds128(smem, xl[1] + 128); SB;
        const f32x4 s1_0 = mfma16(ONES, xt0, ZERO4); SB;
        const f32x4 xn3_1 = smf(CS[6], cb3_1, cb3_2, xn3_0, sidx); SB;
        const u32x4 cb5_1 = lds128(smem, xl[1] + 160); SB;
        const f32x4 xn2_2 = smf(CS[5], cb2_3, cb2_4, xn2_1, sidx); SB;
        const u32x4 cb4_2 = lds128(smem, xl[2] + 128); SB;
        const u32x4 cb5_2 = lds128(smem, xl[2] + 160); SB;
        const f32x4 xn3_2 = smf(CS[7], cb3_3, cb3_4, xn3_1, sidx); SB;
        const u32x4 cb4_3 = lds128(smem, xl[3] + 128); SB;
        const u32x4 cb5_3 = lds128(smem, xl[3] + 160); SB;
        const unsigned xb1_0 = pk2(xn2_2[0], xn2_2[1]); SB;
        const unsigned xb1_1 = pk2(xn2_2[2], xn2_2[3]); SB;
        const unsigned xb1_2 = pk2(xn3_2[0], xn3_2[1]); SB;
        const unsigned xb1_3 = pk2(xn3_2[2], xn3_2[3]); SB;
        const u32x4 xt1 = u32x4{xb1_0, xb1_1, xb1_2, xb1_3}; XLn[1] = xt1;
        const f32x4 s2_1 = mfma16(xt1, xt1, s2_0); SB;
        const f32x4 s1_1 = mfma16(ONES, xt1, s1_0); SB;
        const u32x4 cb4_4 = lds128(smem, xl[4] + 128); SB;
        const u32x4 cb5_4 = lds128(smem, xl[4] + 160); SB;
        const f32x4 xn4_0 = mfma16(CD[4], cb4_0, ZERO4); SB;
        const f32x4 xn4_1 = smf(CS[8], cb4_1, cb4_2, xn4_0, sidx); SB;
        const f32x4 xn4_2 = smf(CS[9], cb4_3, cb4_4, xn4_1, sidx); SB;
        const f32x4 xn5_0 = mfma16(CD[5], cb5_0, ZERO4); SB;
        const f32x4 xn5_1 = smf(CS[10], cb5_1, cb5_2, xn5_0, sidx); SB;
        const f32x4 xn5_2 = smf(CS[11], cb5_3, cb5_4, xn5_1, sidx); SB;
        const unsigned xb2_0 = pk2(xn4_2[0], xn4_2[1]); SB;
        const unsigned xb2_1 = pk2(xn4_2[2], xn4_2[3]); SB;
        const unsigned xb2_2 = pk2(xn5_2[0], xn5_2[1]); SB;
        const unsigned xb2_3 = pk2(xn5_2[2], xn5_2[3]); SB;
        const u32x4 xt2 = u32x4{xb2_0, xb2_1, xb2_2, xb2_3}; XLn[2] = xt2;
        const f32x4 s2_2 = mfma16(xt2, xt2, s2_1); SB;
        const f32x4 s1_2 = mfma16(ONES, xt2, s1_1); SB;
        const float d0 = dm0 ? s2_2[0] : 0.0f; SB;
        const float d1 = dm1 ? s2_2[1] : d0; SB;
        const float d2 = dm2 ? s2_2[2] : d1; SB;
        const float d3 = dm3 ? s2_2[3] : d2; SB;
        const float mean = s1_2[0] * (1.0f / 96.0f); SB;
        const float tv = fmaf(-mean, mean, 1e-5f); SB;
        const f32x4 sq = __builtin_amdgcn_mfma_f32_16x16x4f32(1.0f, d3, ZERO4, 0, 0, 0); SB;
        const float var = fmaf(sq[0], 1.0f / 96.0f, tv); SB;
        const float varc = __builtin_amdgcn_fmed3f(var, 1e-5f, 3.0e38f); SB;
        const float rstd = __builtin_amdgcn_rsqf(varc); SB;
        const float nm = -mean * rstd; SB;
        const float h0_0_0 = fmaf(xn0_2[0], rstd, nm); SB;
        const float h0_0_1 = fmaf(xn0_2[1], rstd, nm); SB;
        const float h0_1_0 = fmaf(xn0_2[2], rstd, nm); SB;
        const float h0_1_1 = fmaf(xn0_2[3], rstd, nm); SB;
        const float h0_2_0 = fmaf(xn1_2[0], rstd, nm); SB;
        const float h0_2_1 = fmaf(xn1_2[1], rstd, nm); SB;
        const float h0_3_0 = fmaf(xn1_2[2], rstd, nm); SB;
        const float h0_3_1 = fmaf(xn1_2[3], rstd, nm); SB;
        const float h1_0_0 = fmaf(xn2_2[0], rstd, nm); SB;
        const float h1_0_1 = fmaf(xn2_2[1], rstd, nm); SB;
        const float h1_1_0 = fmaf(xn2_2[2], rstd, nm); SB;
        const float h1_1_1 = fmaf(xn2_2[3], rstd, nm); SB;
        const float h1_2_0 = fmaf(xn3_2[0], rstd, nm); SB;
        const float h1_2_1 = fmaf(xn3_2[1], rstd, nm); SB;
        const float h1_3_0 = fmaf(xn3_2[2], rstd, nm); SB;
        const float h1_3_1 = fmaf(xn3_2[3], rstd, nm); SB;
        const float h2_0_0 = fmaf(xn4_2[0], rstd, nm); SB;
        const float h2_0_1 = fmaf(xn4_2[1], rstd, nm); SB;
        const float h2_1_0 = fmaf(xn4_2[2], rstd, nm); SB;
        const float h2_1_1 = fmaf(xn4_2[3], rstd, nm); SB;
        const float h2_2_0 = fmaf(xn5_2[0], rstd, nm); SB;
        const float h2_2_1 = fmaf(xn5_2[1], rstd, nm); SB;
        const float h2_3_0 = fmaf(xn5_2[2], rstd, nm); SB;
        const float h2_3_1 = fmaf(xn5_2[3], rstd, nm); SB;
        const unsigned hv0_0 = pk2(h0_0_0, h0_0_1); SB;
        const unsigned hv0_1 = pk2(h0_1_0, h0_1_1); SB;
        const unsigned hv0_2 = pk2(h0_2_0, h0_2_1); SB;
        const unsigned hv0_3 = pk2(h0_3_0, h0_3_1); SB;
        const unsigned hv1_0 = pk2(h1_0_0, h1_0_1); SB;
        const unsigned hv1_1 = pk2(h1_1_0, h1_1_1); SB;
        const unsigned hv1_2 = pk2(h1_2_0, h1_2_1); SB;
        const unsigned hv1_3 = pk2(h1_3_0, h1_3_1); SB;
        const unsigned hv2_0 = pk2(h2_0_0, h2_0_1); SB;
        const unsigned hv2_1 = pk2(h2_1_0, h2_1_1); SB;
        const unsigned hv2_2 = pk2(h2_2_0, h2_2_1); SB;
        const unsigned hv2_3 = pk2(h2_3_0, h2_3_1); SB;
        *(u32x4*)(smem + hlw + 0) = u32x4{hv0_0, hv0_1, hv0_2, hv0_3}; SB;
        *(u32x4*)(smem + hlw + 1024) = u32x4{hv1_0, hv1_1, hv1_2, hv1_3}; SB;
        *(u32x4*)(smem + hlw + 2048) = u32x4{hv2_0, hv2_1, hv2_2, hv2_3}; SB;
      }
      STAMP(2);
      SB;
      wo_stage(ldsbase, wo, 0, lane); wo_stage(ldsbase, wo, 1, lane); wo_stage(ldsbase, wo, 2, lane);
      TS(6);
      BAR_LGKM();
      __builtin_amdgcn_s_setprio(P2_A);
      TS(7);
      STAMP(5);
      tile4(IC<2>{}, cur_at(2));
      xl_one(ldsbase + 3 * XLS, cur_at(7), true);
#pragma unroll
      for (int u = 0; u < 3; ++u) {
        const u32x4 r = lds128(smem, WO + 1024 + u * 1024 + l16);
#pragma unroll
        for (int q = 0; q < 4; ++q) rel[4 * u + q] = r[q];
      }
#pragma unroll
      for (int q = 0; q < 3; ++q) rel[12 + q] = rel4[q];
      asm volatile("" : "+v"(rel[0]), "+v"(rel[1]), "+v"(rel[2]), "+v"(rel[3]), "+v"(rel[4]), "+v"(rel[5]), "+v"(rel[6]), "+v"(rel[7]),
                        "+v"(rel[8]), "+v"(rel[9]), "+v"(rel[10]), "+v"(rel[11]), "+v"(rel[12]), "+v"(rel[13]), "+v"(rel[14]));
      WAIT_VM(6);
#pragma unroll
      for (int bp = 0; bp < 4; ++bp) {
        u32x4 f0, f1, f2;
        wo_frags(smem, bp, g, t, f0, f1, f2);
        *(u32x4*)(smem + wob + (bp * 3 + 0) * 1024) = f0;
        *(u32x4*)(smem + wob + (bp * 3 + 1) * 1024) = f1;
        *(u32x4*)(smem + wob + (bp * 3 + 2) * 1024) = f2;
      }
#pragma unroll
      for (int h = 0; h < 2; ++h) wo_frags(smem, 4 + h, g, t, W45[3 * h], W45[3 * h + 1], W45[3 * h + 2]);
      STAMP(6);
      hlw ^= (unsigned)(HL0 ^ HL1);
      TS(8);
      BAR_VM(0);
      TS(9);
      TSOUT();
      STAMP(1);
      __builtin_amdgcn_s_setprio(C_PRIO);
      int ocx = wstart % NWS;
      unsigned obase = (unsigned)(((wstart / NWS) * 7 * IMG + ocx * 7) * PIXB);
      obase -= ocx < PERX ? STEP_WRAP : STEP_IN;
      ocx += ocx < PERX ? NWS - PERX : -PERX;
      Cur lc = cur_at(0);
      int lm = 0;
      auto set_orelL = [&]() {
        unsigned o = OOB;
#pragma unroll
        for (int j = 0; j < 4; ++j) {
          const unsigned ob = lm < nwin ? lc.base + (unsigned)(6 * IMG * PIXB) : OOB;
          o = t == j ? ob + g * 16 : o;
          cur_next(lc); ++lm;
        }
        orelL = o;
      };
      auto stepC = [&](auto VC) {
        constexpr int V = decltype(VC)::value;
        constexpr int OOFF = ((V + 1) & 1) * 9216;
        constexpr int QOFF = ((V + 1) & 1) * 3072;
        const __amdgpu_buffer_rsrc_t psrd = osrd;
        if constexpr (V == 0) set_orelL();
        SB;
        if constexpr (V == 0) {
        const f32x4 pil0 = mfma16(SEL0, XLc[0], ZERO4); SB;
        const u32x4 xq0 = lds128(smem, rq + QOFF + 0); SB;
        const u32x4 of2_0 = lds128(smem, ro + OOFF + 6144); SB;
        const f32x4 pil3 = mfma16(SEL1, XLc[1], ZERO4); SB;
        const u32x4 xq2 = lds128(smem, rq + QOFF + 2048); SB;
        const u32x4 xq1 = lds128(smem, rq + QOFF + 1024); SB;
        const f32x4 pil1 = mfma16(SEL1, XLc[0], ZERO4); SB;
        const u32x4 of0_0 = lds128(smem, ro + OOFF + 0); SB;
        const f32x4 pil2 = mfma16(SEL0, XLc[1], ZERO4); SB;
        const u32x4 of1_0 = lds128(smem, ro + OOFF + 3072); SB;
        const u32x4 ol0 = lds128(smem, rol + 0); SB;
        const f32x4 pil4 = mfma16(SEL0, XLc[2], ZERO4); SB;
        const u32x4 wo0_0 = lds128(smem, wob + 0); SB;
        const u32x4 ol1 = lds128(smem, rol + 1024); SB;
        const f32x4 pil5 = mfma16(SEL1, XLc[2], ZERO4); SB;
        const u32x4 ol2 = lds128(smem, rol + 2048); SB;
        const f32x4 pl4_0 = mfma16(W45[0], ol0, pil4); SB;
        const f32x4 pl0_0 = mfma16(wo0_0, ol0, pil0); SB;
        const f32x4 pl4_1 = mfma16(W45[1], ol1, pl4_0); SB;
        const f32x4 pl5_0 = mfma16(W45[3], ol0, pil5); SB;
        const f32x4 pl5_1 = mfma16(W45[4], ol1, pl5_0); SB;
        const f32x4 pl5_2 = mfma16(W45[5], ol2, pl5_1); SB;
        const f32x4 pl4_2 = mfma16(W45[2], ol2, pl4_1); SB;
        const unsigned pol4_0 = pk2(pl4_2[0], pl4_2[1]); SB;
        const unsigned pol4_1 = pk2(pl4_2[2], pl4_2[3]); SB;
        const unsigned pol5_0 = pk2(pl5_2[0], pl5_2[1]); SB;
        const unsigned pol5_1 = pk2(pl5_2[2], pl5_2[3]); SB;
        const u32x4 wo0_1 = lds128(smem, wob + 1024); SB;
        const f32x4 pic0_4 = mfma16(SEL0, xq0, ZERO4); SB;
        const f32x4 pl0_1 = mfma16(wo0_1, ol1, pl0_0); SB;
        const u32x4 wo0_2 = lds128(smem, wob + 2048); SB;
        __builtin_amdgcn_raw_buffer_store_b128(u32x4{pol4_0, pol4_1, pol5_0, pol5_1}, osrd, orelL, 128, ST_AUX); SB;
        const f32x4 pc0_4_0 = mfma16(W45[0], of0_0, pic0_4); SB;
        const f32x4 pl0_2 = mfma16(wo0_2, ol2, pl0_1); SB;
        const unsigned pol0_0 = pk2(pl0_2[0], pl0_2[1]); SB;
        const unsigned pol0_1 = pk2(pl0_2[2], pl0_2[3]); SB;
        const f32x4 pic0_5 = mfma16(SEL1, xq0, ZERO4); SB;
        const u32x4 wo1_0 = lds128(smem, wob + 3072); SB;
        const f32x4 pc0_5_0 = mfma16(W45[3], of0_0, pic0_5); SB;
        const u32x4 wo1_1 = lds128(smem, wob + 4096); SB;
        const u32x4 wo1_2 = lds128(smem, wob + 5120); SB;
        const f32x4 pl1_0 = mfma16(wo1_0, ol0, pil1); SB;
        const f32x4 pl1_1 = mfma16(wo1_1, ol1, pl1_0); SB;
        const u32x4 wo2_0 = lds128(smem, wob + 6144); SB;
        const f32x4 pl1_2 = mfma16(wo1_2, ol2, pl1_1); SB;
        const u32x4 wo2_1 = lds128(smem, wob + 7168); SB;
        const u32x4 wo2_2 = lds128(smem, wob + 8192); SB;
        const unsigned pol1_0 = pk2(pl1_2[0], pl1_2[1]); SB;
        const unsigned pol1_1 = pk2(pl1_2[2], pl1_2[3]); SB;
        const f32x4 pl2_0 = mfma16(wo2_0, ol0, pil2); SB;
        __builtin_amdgcn_raw_buffer_store_b128(u32x4{pol0_0, pol0_1, pol1_0, pol1_1}, osrd, orelL, 0, ST_AUX); SB;
        const f32x4 pic1_4 = mfma16(SEL0, xq1, ZERO4); SB;
        const f32x4 pc1_4_0 = mfma16(W45[0], of1_0, pic1_4); SB;
        const f32x4 pl2_1 = mfma16(wo2_1, ol1, pl2_0); SB;
        const u32x4 wo3_0 = lds128(smem, wob + 9216); SB;
        const f32x4 pl2_2 = mfma16(wo2_2, ol2, pl2_1); SB;
        const u32x4 of0_1 = lds128(smem, ro + OOFF + 1024); SB;
        const u32x4 of1_1 = lds128(smem, ro + OOFF + 4096); SB;
        const unsigned pol2_0 = pk2(pl2_2[0], pl2_2[1]); SB;
        const unsigned pol2_1 = pk2(pl2_2[2], pl2_2[3]); SB;
        const f32x4 pic1_5 = mfma16(SEL1, xq1, ZERO4); SB;
        const f32x4 pc1_5_0 = mfma16(W45[3], of1_0, pic1_5); SB;
        const u32x4 of2_1 = lds128(smem, ro + OOFF + 7168); SB;
        const f32x4 pl3_0 = mfma16(wo3_0, ol0, pil3); SB;
        const u32x4 wo3_1 = lds128(smem, wob + 10240); SB;
        const u32x4 of0_2 = lds128(smem, ro + OOFF + 2048); SB;
        const f32x4 pc0_4_1 = mfma16(W45[1], of0_1, pc0_4_0); SB;
        const f32x4 pc0_5_1 = mfma16(W45[4], of0_1, pc0_5_0); SB;
        const u32x4 of1_2 = lds128(smem, ro + OOFF + 5120); SB;
        const f32x4 pc1_4_1 = mfma16(W45[1], of1_1, pc1_4_0); SB;
        const f32x4 pc1_5_1 = mfma16(W45[4], of1_1, pc1_5_0); SB;
        const f32x4 pl3_1 = mfma16(wo3_1, ol1, pl3_0); SB;
        const u32x4 of2_2 = lds128(smem, ro + OOFF + 8192); SB;
        const u32x4 wo3_2 = lds128(smem, wob + 11264); SB;
        const f32x4 pic2_4 = mfma16(SEL0, xq2, ZERO4); SB;
        const f32x4 pc2_4_0 = mfma16(W45[0], of2_0, pic2_4); SB;
        const f32x4 pc2_4_1 = mfma16(W45[1], of2_1, pc2_4_0); SB;
        const f32x4 pc0_4_2 = mfma16(W45[2], of0_2, pc0_4_1); SB;
        const f32x4 pc0_5_2 = mfma16(W45[5], of0_2, pc0_5_1); SB;
        __builtin_amdgcn_s_setprio(3); SB;
        const f32x4 pic2_5 = mfma16(SEL1, xq2, ZERO4); SB;
        const f32x4 pc2_5_0 = mfma16(W45[3], of2_0, pic2_5); SB;
        const f32x4 pc1_5_2 = mfma16(W45[5], of1_2, pc1_5_1); SB;
        const unsigned poc0_5_1 = pk2(pc0_5_2[2], pc0_5_2[3]); SB;
        const unsigned poc0_4_1 = pk2(pc0_4_2[2], pc0_4_2[3]); SB;
        const unsigned poc0_5_0 = pk2(pc0_5_2[0], pc0_5_2[1]); SB;
        const f32x4 pc2_5_1 = mfma16(W45[4], of2_1, pc2_5_0); SB;
        const f32x4 pc1_4_2 = mfma16(W45[2], of1_2, pc1_4_1); SB;
        const unsigned poc0_4_0 = pk2(pc0_4_2[0], pc0_4_2[1]); SB;
        __builtin_amdgcn_raw_buffer_store_b128(u32x4{poc0_4_0, poc0_4_1, poc0_5_0, poc0_5_1}, psrd, orel[0], obase + 128, ST_AUX); SB;
        const unsigned poc1_4_0 = pk2(pc1_4_2[0], pc1_4_2[1]); SB;
        const unsigned poc1_4_1 = pk2(pc1_4_2[2], pc1_4_2[3]); SB;
        const unsigned poc1_5_0 = pk2(pc1_5_2[0], pc1_5_2[1]); SB;
        const unsigned poc1_5_1 = pk2(pc1_5_2[2], pc1_5_2[3]); SB;
        const f32x4 pc2_4_2 = mfma16(W45[2], of2_2, pc2_4_1); SB;
        const f32x4 pc2_5_2 = mfma16(W45[5], of2_2, pc2_5_1); SB;
        const f32x4 pl3_2 = mfma16(wo3_2, ol2, pl3_1); SB;
        __builtin_amdgcn_raw_buffer_store_b128(u32x4{poc1_4_0, poc1_4_1, poc1_5_0, poc1_5_1}, psrd, orel[1], obase + 128, ST_AUX); SB;
        const unsigned poc2_4_0 = pk2(pc2_4_2[0], pc2_4_2[1]); SB;
        const unsigned poc2_5_0 = pk2(pc2_5_2[0], pc2_5_2[1]); SB;
        const unsigned poc2_4_1 = pk2(pc2_4_2[2], pc2_4_2[3]); SB;
        const unsigned poc2_5_1 = pk2(pc2_5_2[2], pc2_5_2[3]); SB;
        const unsigned pol3_0 = pk2(pl3_2[0], pl3_2[1]); SB;
        __builtin_amdgcn_raw_buffer_store_b128(u32x4{poc2_4_0, poc2_4_1, poc2_5_0, poc2_5_1}, psrd, orel[2], obase + 128, ST_AUX); SB;
        const unsigned pol3_1 = pk2(pl3_2[2], pl3_2[3]); SB;
        __builtin_amdgcn_raw_buffer_store_b128(u32x4{pol2_0, pol2_1, pol3_0, pol3_1}, osrd, orelL, 64, ST_AUX); SB;
        dma_part(IC<V>{}, IC<0>{}, IC<15>{}, ldsbase); SB;
        __builtin_amdgcn_s_setprio(2); SB;
        } else if constexpr (V == 1) {
        const u32x4 xq0 = lds128(smem, rq + QOFF + 0); SB;
        const u32x4 xq2 = lds128(smem, rq + QOFF + 2048); SB;
        const f32x4 pic2_4 = mfma16(SEL0, xq2, ZERO4); SB;
        const u32x4 xq1 = lds128(smem, rq + QOFF + 1024); SB;
        const f32x4 pic1_5 = mfma16(SEL1, xq1, ZERO4); SB;
        const u32x4 of0_2 = lds128(smem, ro + OOFF + 2048); SB;
        const u32x4 of2_1 = lds128(smem, ro + OOFF + 7168); SB;
        const f32x4 pic2_5 = mfma16(SEL1, xq2, ZERO4); SB;
        const u32x4 of2_0 = lds128(smem, ro + OOFF + 6144); SB;
        const u32x4 of1_1 = lds128(smem, ro + OOFF + 4096); SB;
        const u32x4 of1_0 = lds128(smem, ro + OOFF + 3072); SB;
        const f32x4 pic1_4 = mfma16(SEL0, xq1, ZERO4); SB;
        dma_part(IC<V>{}, IC<0>{}, IC<15>{}, ldsbase); SB;
        const u32x4 of0_0 = lds128(smem, ro + OOFF + 0); SB;
        const u32x4 of0_1 = lds128(smem, ro + OOFF + 1024); SB;
        const u32x4 cb0_0 = lds128(smem, xl[0] + 0); SB;
        const f32x4 pic0_5 = mfma16(SEL1, xq0, ZERO4); SB;
        const f32x4 pc0_5_0 = mfma16(W45[3], of0_0, pic0_5); SB;
        const u32x4 cb1_0 = lds128(smem, xl[0] + 32); SB;
        const f32x4 pc2_4_0 = mfma16(W45[0], of2_0, pic2_4); SB;
        const f32x4 pc2_5_0 = mfma16(W45[3], of2_0, pic2_5); SB;
        const f32x4 pc1_4_0 = mfma16(W45[0], of1_0, pic1_4); SB;
        const f32x4 pc1_5_0 = mfma16(W45[3], of1_0, pic1_5); SB;
        const u32x4 cb0_1 = lds128(smem, xl[1] + 0); SB;
        const u32x4 cb1_1 = lds128(smem, xl[1] + 32); SB;
        const f32x4 pc1_5_1 = mfma16(W45[4], of1_1, pc1_5_0); SB;
        const f32x4 pc1_4_1 = mfma16(W45[1], of1_1, pc1_4_0); SB;
        const f32x4 pc2_5_1 = mfma16(W45[4], of2_1, pc2_5_0); SB;
        const f32x4 pc0_5_1 = mfma16(W45[4], of0_1, pc0_5_0); SB;
        const f32x4 pc0_5_2 = mfma16(W45[5], of0_2, pc0_5_1); SB;
        const f32x4 pc2_4_1 = mfma16(W45[1], of2_1, pc2_4_0); SB;
        const u32x4 cb0_2 = lds128(smem, xl[2] + 0); SB;
        const unsigned poc0_5_0 = pk2(pc0_5_2[0], pc0_5_2[1]); SB;
        const unsigned poc0_5_1 = pk2(pc0_5_2[2], pc0_5_2[3]); SB;
        const f32x4 pic0_4 = mfma16(SEL0, xq0, ZERO4); SB;
        const u32x4 cb1_2 = lds128(smem, xl[2] + 32); SB;
        const f32x4 pc0_4_0 = mfma16(W45[0], of0_0, pic0_4); SB;
        const u32x4 cb0_3 = lds128(smem, xl[3] + 0); SB;
        const u32x4 cb1_3 = lds128(smem, xl[3] + 32); SB;
        const f32x4 pc0_4_1 = mfma16(W45[1], of0_1, pc0_4_0); SB;
        const u32x4 cb0_4 = lds128(smem, xl[4] + 0); SB;
        const f32x4 pc0_4_2 = mfma16(W45[2], of0_2, pc0_4_1); SB;
        const unsigned poc0_4_1 = pk2(pc0_4_2[2], pc0_4_2[3]); SB;
        const f32x4 xn0_0 = mfma16(CD[0], cb0_0, ZERO4); SB;
        const unsigned poc0_4_0 = pk2(pc0_4_2[0], pc0_4_2[1]); SB;
        __builtin_amdgcn_raw_buffer_store_b128(u32x4{poc0_4_0, poc0_4_1, poc0_5_0, poc0_5_1}, psrd, orel[0], obase + 128, ST_AUX); SB;
        const u32x4 cb1_4 = lds128(smem, xl[4] + 32); SB;
        const u32x4 cb2_0 = lds128(smem, xl[0] + 64); SB;
        const u32x4 cb3_0 = lds128(smem, xl[0] + 96); SB;
        const f32x4 xn0_1 = smf(CS[0], cb0_1, cb0_2, xn0_0, sidx); SB;
        const f32x4 xn0_2 = smf(CS[1], cb0_3, cb0_4, xn0_1, sidx); xp[0] = xn0_2; SB;
        const unsigned xb0_1 = pk2(xn0_2[2], xn0_2[3]); SB;
        const unsigned xb0_0 = pk2(xn0_2[0], xn0_2[1]); SB;
        const f32x4 xn1_0 = mfma16(CD[1], cb1_0, ZERO4); SB;
        const u32x4 cb2_1 = lds128(smem, xl[1] + 64); SB;
        const u32x4 cb3_1 = lds128(smem, xl[1] + 96); SB;
        __builtin_amdgcn_s_setprio(3); SB;
        const u32x4 cb2_2 = lds128(smem, xl[2] + 64); SB;
        const f32x4 xn1_1 = smf(CS[2], cb1_1, cb1_2, xn1_0, sidx); SB;
        const u32x4 cb3_2 = lds128(smem, xl[2] + 96); SB;
        const u32x4 cb2_3 = lds128(smem, xl[3] + 64); SB;
        const f32x4 xn1_2 = smf(CS[3], cb1_3, cb1_4, xn1_1, sidx); xp[1] = xn1_2; SB;
        const u32x4 cb3_3 = lds128(smem, xl[3] + 96); SB;
        const u32x4 cb2_4 = lds128(smem, xl[4] + 64); SB;
        const unsigned xb0_2 = pk2(xn1_2[0], xn1_2[1]); SB;
        const f32x4 xn2_0 = mfma16(CD[2], cb2_0, ZERO4); SB;
        const u32x4 cb3_4 = lds128(smem, xl[4] + 96); SB;
        const unsigned xb0_3 = pk2(xn1_2[2], xn1_2[3]); SB;
        const u32x4 xt0 = u32x4{xb0_0, xb0_1, xb0_2, xb0_3}; XLn[0] = xt0;
        const f32x4 xn2_1 = smf(CS[4], cb2_1, cb2_2, xn2_0, sidx); SB;
        const u32x4 cb4_0 = lds128(smem, xl[0] + 128); SB;
        const u32x4 cb5_0 = lds128(smem, xl[0] + 160); SB;
        const f32x4 xn2_2 = smf(CS[5], cb2_3, cb2_4, xn2_1, sidx); xp[2] = xn2_2; SB;
        const u32x4 of1_2 = lds128(smem, ro + OOFF + 5120); SB;
        const unsigned xb1_0 = pk2(xn2_2[0], xn2_2[1]); SB;
        const unsigned xb1_1 = pk2(xn2_2[2], xn2_2[3]); SB;
        const u32x4 cb4_1 = lds128(smem, xl[1] + 128); SB;
        const f32x4 xn3_0 = mfma16(CD[3], cb3_0, ZERO4); SB;
        const u32x4 cb5_1 = lds128(smem, xl[1] + 160); SB;
        const f32x4 xn3_1 = smf(CS[6], cb3_1, cb3_2, xn3_0, sidx); SB;
        const u32x4 cb4_2 = lds128(smem, xl[2] + 128); SB;
        const u32x4 cb5_2 = lds128(smem, xl[2] + 160); SB;
        const u32x4 cb4_3 = lds128(smem, xl[3] + 128); SB;
        const f32x4 xn3_2 = smf(CS[7], cb3_3, cb3_4, xn3_1, sidx); xp[3] = xn3_2; SB;
        const u32x4 of2_2 = lds128(smem, ro + OOFF + 8192); SB;
        const f32x4 pc1_4_2 = mfma16(W45[2], of1_2, pc1_4_1); SB;
        const f32x4 pc1_5_2 = mfma16(W45[5], of1_2, pc1_5_1); SB;
        const u32x4 cb5_3 = lds128(smem, xl[3] + 160); SB;
        const unsigned xb1_3 = pk2(xn3_2[2], xn3_2[3]); SB;
        const unsigned xb1_2 = pk2(xn3_2[0], xn3_2[1]); SB;
        const u32x4 xt1 = u32x4{xb1_0, xb1_1, xb1_2, xb1_3}; XLn[1] = xt1;
        const f32x4 xn4_0 = mfma16(CD[4], cb4_0, ZERO4); SB;
        const u32x4 cb4_4 = lds128(smem, xl[4] + 128); SB;
        const unsigned poc1_4_0 = pk2(pc1_4_2[0], pc1_4_2[1]); SB;
        const f32x4 xn4_1 = smf(CS[8], cb4_1, cb4_2, xn4_0, sidx); SB;
        const u32x4 cb5_4 = lds128(smem, xl[4] + 160); SB;
        const unsigned poc1_5_0 = pk2(pc1_5_2[0], pc1_5_2[1]); SB;
        const f32x4 xn5_0 = mfma16(CD[5], cb5_0, ZERO4); SB;
        const unsigned poc1_5_1 = pk2(pc1_5_2[2], pc1_5_2[3]); SB;
        const f32x4 xn5_1 = smf(CS[10], cb5_1, cb5_2, xn5_0, sidx); SB;
        const unsigned poc1_4_1 = pk2(pc1_4_2[2], pc1_4_2[3]); SB;
        __builtin_amdgcn_raw_buffer_store_b128(u32x4{poc1_4_0, poc1_4_1, poc1_5_0, poc1_5_1}, psrd, orel[1], obase + 128, ST_AUX); SB;
        const f32x4 pc2_5_2 = mfma16(W45[5], of2_2, pc2_5_1); SB;
        const f32x4 xn5_2 = smf(CS[11], cb5_3, cb5_4, xn5_1, sidx); xp[5] = xn5_2; SB;
        const unsigned poc2_5_0 = pk2(pc2_5_2[0], pc2_5_2[1]); SB;
        const f32x4 pc2_4_2 = mfma16(W45[2], of2_2, pc2_4_1); SB;
        const unsigned poc2_4_1 = pk2(pc2_4_2[2], pc2_4_2[3]); SB;
        const unsigned poc2_4_0 = pk2(pc2_4_2[0], pc2_4_2[1]); SB;
        const f32x4 xn4_2 = smf(CS[9], cb4_3, cb4_4, xn4_1, sidx); xp[4] = xn4_2; SB;
        const unsigned poc2_5_1 = pk2(pc2_5_2[2], pc2_5_2[3]); SB;
        const unsigned xb2_0 = pk2(xn4_2[0], xn4_2[1]); SB;
        const unsigned xb2_2 = pk2(xn5_2[0], xn5_2[1]); SB;
        const unsigned xb2_3 = pk2(xn5_2[2], xn5_2[3]); SB;
        __builtin_amdgcn_raw_buffer_store_b128(u32x4{poc2_4_0, poc2_4_1, poc2_5_0, poc2_5_1}, psrd, orel[2], obase + 128, ST_AUX); SB;
        const unsigned xb2_1 = pk2(xn4_2[2], xn4_2[3]); SB;
        const u32x4 xt2 = u32x4{xb2_0, xb2_1, xb2_2, xb2_3}; XLn[2] = xt2;
        __builtin_amdgcn_s_setprio(2); SB;
        } else if constexpr (V == 2) {
        dma_part(IC<V>{}, IC<0>{}, IC<15>{}, ldsbase); SB;
        const f32x4 s2_0 = mfma16(XLn[0], XLn[0], ZERO4); SB;
        const u32x4 of0_0 = lds128(smem, ro + OOFF + 0); SB;
        const u32x4 xq0 = lds128(smem, rq + QOFF + 0); SB;
        const u32x4 xq1 = lds128(smem, rq + QOFF + 1024); SB;
        const f32x4 s2_1 = mfma16(XLn[1], XLn[1], s2_0); SB;
        const u32x4 xq2 = lds128(smem, rq + QOFF + 2048); SB;
        const f32x4 s2_2 = mfma16(XLn[2], XLn[2], s2_1); SB;
        const u32x4 of1_0 = lds128(smem, ro + OOFF + 3072); SB;
        const f32x4 s1_0 = mfma16(ONES, XLn[0], ZERO4); SB;
        const f32x4 s1_1 = mfma16(ONES, XLn[1], s1_0); SB;
        const u32x4 of0_1 = lds128(smem, ro + OOFF + 1024); SB;
        const u32x4 of1_1 = lds128(smem, ro + OOFF + 4096); SB;
        const u32x4 of2_1 = lds128(smem, ro + OOFF + 7168); SB;
        const f32x4 s1_2 = mfma16(ONES, XLn[2], s1_1); SB;
        const float d0 = dm0 ? s2_2[0] : 0.0f; SB;
        const float d1 = dm1 ? s2_2[1] : d0; SB;
        const float d2 = dm2 ? s2_2[2] : d1; SB;
        const u32x4 of1_2 = lds128(smem, ro + OOFF + 5120); SB;
        const float d3 = dm3 ? s2_2[3] : d2; SB;
        const u32x4 of0_2 = lds128(smem, ro + OOFF + 2048); SB;
        const f32x4 sq = __builtin_amdgcn_mfma_f32_16x16x4f32(1.0f, d3, ZERO4, 0, 0, 0); SB;
        __builtin_amdgcn_s_setprio(3); SB;
        const float mean = s1_2[0] * (1.0f / 96.0f); SB;
        const float tv = fmaf(-mean, mean, 1e-5f); SB;
        const u32x4 of2_2 = lds128(smem, ro + OOFF + 8192); SB;
        const f32x4 pic0_4 = mfma16(SEL0, xq0, ZERO4); SB;
        const u32x4 of2_0 = lds128(smem, ro + OOFF + 6144); SB;
        const f32x4 pc0_4_0 = mfma16(W45[0], of0_0, pic0_4); SB;
        const f32x4 pc0_4_1 = mfma16(W45[1], of0_1, pc0_4_0); SB;
        const float var = fmaf(sq[0], 1.0f / 96.0f, tv); SB;
        const float varc = __builtin_amdgcn_fmed3f(var, 1e-5f, 3.0e38f); SB;
        const f32x4 pic0_5 = mfma16(SEL1, xq0, ZERO4); SB;
        const float rstd = __builtin_amdgcn_rsqf(varc); SB;
        const f32x4 pic1_4 = mfma16(SEL0, xq1, ZERO4); SB;
        const float nm = -mean * rstd; SB;
        const float h0_1_1 = fmaf(xp[0][3], rstd, nm); SB;
        const float h0_2_0 = fmaf(xp[1][0], rstd, nm); SB;
        const float h1_3_0 = fmaf(xp[3][2], rstd, nm); SB;
        const f32x4 pc0_5_0 = mfma16(W45[3], of0_0, pic0_5); SB;
        const float h0_0_1 = fmaf(xp[0][1], rstd, nm); SB;
        const float h0_0_0 = fmaf(xp[0][0], rstd, nm); SB;
        const float h0_1_0 = fmaf(xp[0][2], rstd, nm); SB;
        const f32x4 pc1_4_0 = mfma16(W45[0], of1_0, pic1_4); SB;
        const f32x4 pc0_5_1 = mfma16(W45[4], of0_1, pc0_5_0); SB;
        const float h0_2_1 = fmaf(xp[1][1], rstd, nm); SB;
        const float h0_3_0 = fmaf(xp[1][2], rstd, nm); SB;
        const float h2_3_0 = fmaf(xp[5][2], rstd, nm); SB;
        const float h0_3_1 = fmaf(xp[1][3], rstd, nm); SB;
        const float h2_2_0 = fmaf(xp[5][0], rstd, nm); SB;
        const float h2_1_1 = fmaf(xp[4][3], rstd, nm); SB;
        const unsigned hv0_2 = pk2(h0_2_0, h0_2_1); SB;
        const float h1_0_0 = fmaf(xp[2][0], rstd, nm); SB;
        const float h1_0_1 = fmaf(xp[2][1], rstd, nm); SB;
        const unsigned hv0_1 = pk2(h0_1_0, h0_1_1); SB;
        const float h1_1_0 = fmaf(xp[2][2], rstd, nm); SB;
        const float h1_1_1 = fmaf(xp[2][3], rstd, nm); SB;
        const float h1_2_0 = fmaf(xp[3][0], rstd, nm); SB;
        const float h1_2_1 = fmaf(xp[3][1], rstd, nm); SB;
        const float h2_0_0 = fmaf(xp[4][0], rstd, nm); SB;
        const float h2_0_1 = fmaf(xp[4][1], rstd, nm); SB;
        __builtin_amdgcn_s_setprio(1); SB;
        const float h2_3_1 = fmaf(xp[5][3], rstd, nm); SB;
        const float h1_3_1 = fmaf(xp[3][3], rstd, nm); SB;
        const float h2_2_1 = fmaf(xp[5][1], rstd, nm); SB;
        const unsigned hv0_0 = pk2(h0_0_0, h0_0_1); SB;
        const float h2_1_0 = fmaf(xp[4][2], rstd, nm); SB;
        const unsigned hv0_3 = pk2(h0_3_0, h0_3_1); SB;
        const unsigned hv2_3 = pk2(h2_3_0, h2_3_1); SB;
        *(u32x4*)(smem + hlw + 0) = u32x4{hv0_0, hv0_1, hv0_2, hv0_3}; SB;
        const f32x4 pic1_5 = mfma16(SEL1, xq1, ZERO4); SB;
        const f32x4 pc1_5_0 = mfma16(W45[3], of1_0, pic1_5); SB;
        const unsigned hv1_0 = pk2(h1_0_0, h1_0_1); SB;
        const unsigned hv1_1 = pk2(h1_1_0, h1_1_1); SB;
        const unsigned hv1_2 = pk2(h1_2_0, h1_2_1); SB;
        const unsigned hv1_3 = pk2(h1_3_0, h1_3_1); SB;
        const f32x4 pic2_5 = mfma16(SEL1, xq2, ZERO4); SB;
        const unsigned hv2_0 = pk2(h2_0_0, h2_0_1); SB;
        const unsigned hv2_1 = pk2(h2_1_0, h2_1_1); SB;
        *(u32x4*)(smem + hlw + 1024) = u32x4{hv1_0, hv1_1, hv1_2, hv1_3}; SB;
        const unsigned hv2_2 = pk2(h2_2_0, h2_2_1); SB;
        const f32x4 pic2_4 = mfma16(SEL0, xq2, ZERO4); SB;
        *(u32x4*)(smem + hlw + 2048) = u32x4{hv2_0, hv2_1, hv2_2, hv2_3}; SB;
        const f32x4 pc2_4_0 = mfma16(W45[0], of2_0, pic2_4); SB;
        const f32x4 pc2_5_0 = mfma16(W45[3], of2_0, pic2_5); SB;
        const f32x4 pc1_4_1 = mfma16(W45[1], of1_1, pc1_4_0); SB;
        const f32x4 pc1_5_1 = mfma16(W45[4], of1_1, pc1_5_0); SB;
        const f32x4 pc2_4_1 = mfma16(W45[1], of2_1, pc2_4_0); SB;
        const f32x4 pc2_5_1 = mfma16(W45[4], of2_1, pc2_5_0); SB;
        const f32x4 pc0_4_2 = mfma16(W45[2], of0_2, pc0_4_1); SB;
        const f32x4 pc0_5_2 = mfma16(W45[5], of0_2, pc0_5_1); SB;
        const f32x4 pc1_4_2 = mfma16(W45[2], of1_2, pc1_4_1); SB;
        const f32x4 pc1_5_2 = mfma16(W45[5], of1_2, pc1_5_1); SB;
        const f32x4 pc2_4_2 = mfma16(W45[2], of2_2, pc2_4_1); SB;
        const unsigned poc0_4_0 = pk2(pc0_4_2[0], pc0_4_2[1]); SB;
        const f32x4 pc2_5_2 = mfma16(W45[5], of2_2, pc2_5_1); SB;
        const unsigned poc0_4_1 = pk2(pc0_4_2[2], pc0_4_2[3]); SB;
        const unsigned poc0_5_0 = pk2(pc0_5_2[0], pc0_5_2[1]); SB;
        const unsigned poc1_4_1 = pk2(pc1_4_2[2], pc1_4_2[3]); SB;
        const unsigned poc1_5_0 = pk2(pc1_5_2[0], pc1_5_2[1]); SB;
        const unsigned poc2_4_0 = pk2(pc2_4_2[0], pc2_4_2[1]); SB;
        const unsigned poc2_4_1 = pk2(pc2_4_2[2], pc2_4_2[3]); SB;
        const unsigned poc1_5_1 = pk2(pc1_5_2[2], pc1_5_2[3]); SB;
        const unsigned poc0_5_1 = pk2(pc0_5_2[2], pc0_5_2[3]); SB;
        const unsigned poc1_4_0 = pk2(pc1_4_2[0], pc1_4_2[1]); SB;
        __builtin_amdgcn_raw_buffer_store_b128(u32x4{poc0_4_0, poc0_4_1, poc0_5_0, poc0_5_1}, psrd, orel[0], obase + 128, ST_AUX); SB;
        const unsigned poc2_5_1 = pk2(pc2_5_2[2], pc2_5_2[3]); SB;
        __builtin_amdgcn_raw_buffer_store_b128(u32x4{poc1_4_0, poc1_4_1, poc1_5_0, poc1_5_1}, psrd, orel[1], obase + 128, ST_AUX); SB;
        const unsigned poc2_5_0 = pk2(pc2_5_2[0], pc2_5_2[1]); SB;
        __builtin_amdgcn_raw_buffer_store_b128(u32x4{poc2_4_0, poc2_4_1, poc2_5_0, poc2_5_1}, psrd, orel[2], obase + 128, ST_AUX); SB;
        __builtin_amdgcn_s_setprio(2); SB;
        } else {
        dma_part(IC<V>{}, IC<0>{}, IC<15>{}, ldsbase); SB;
        xl_next(0); SB;
        xl_next(1); SB;
        xl_next(2); SB;
        xl_next(3); SB;
        const u32x4 xq0 = lds128(smem, rq + QOFF + 0); SB;
        const u32x4 xq2 = lds128(smem, rq + QOFF + 2048); SB;
        const u32x4 of0_0 = lds128(smem, ro + OOFF + 0); SB;
        const u32x4 of2_0 = lds128(smem, ro + OOFF + 6144); SB;
        const u32x4 of2_1 = lds128(smem, ro + OOFF + 7168); SB;
        const u32x4 of0_1 = lds128(smem, ro + OOFF + 1024); SB;
        const f32x4 pic0_4 = mfma16(SEL0, xq0, ZERO4); SB;
        const u32x4 xq1 = lds128(smem, rq + QOFF + 1024); SB;
        const u32x4 of1_2 = lds128(smem, ro + OOFF + 5120); SB;
        const u32x4 of1_0 = lds128(smem, ro + OOFF + 3072); SB;
        const f32x4 pic0_5 = mfma16(SEL1, xq0, ZERO4); SB;
        const u32x4 of1_1 = lds128(smem, ro + OOFF + 4096); SB;
        const f32x4 pic1_4 = mfma16(SEL0, xq1, ZERO4); SB;
        const u32x4 of0_2 = lds128(smem, ro + OOFF + 2048); SB;
        const f32x4 pic2_4 = mfma16(SEL0, xq2, ZERO4); SB;
        const f32x4 pc0_4_0 = mfma16(W45[0], of0_0, pic0_4); SB;
        const f32x4 pc0_5_0 = mfma16(W45[3], of0_0, pic0_5); SB;
        const u32x4 of2_2 = lds128(smem, ro + OOFF + 8192); SB;
        const f32x4 pic1_5 = mfma16(SEL1, xq1, ZERO4); SB;
        const f32x4 pc1_5_0 = mfma16(W45[3], of1_0, pic1_5); SB;
        const f32x4 pc1_5_1 = mfma16(W45[4], of1_1, pc1_5_0); SB;
        const f32x4 pic2_5 = mfma16(SEL1, xq2, ZERO4); SB;
        const f32x4 pc1_4_0 = mfma16(W45[0], of1_0, pic1_4); SB;
        const f32x4 pc0_4_1 = mfma16(W45[1], of0_1, pc0_4_0); SB;
        const f32x4 pc0_5_1 = mfma16(W45[4], of0_1, pc0_5_0); SB;
        const f32x4 pc2_4_0 = mfma16(W45[0], of2_0, pic2_4); SB;
        const f32x4 pc2_5_0 = mfma16(W45[3], of2_0, pic2_5); SB;
        const f32x4 pc1_4_1 = mfma16(W45[1], of1_1, pc1_4_0); SB;
        const f32x4 pc0_5_2 = mfma16(W45[5], of0_2, pc0_5_1); SB;
        const f32x4 pc2_5_1 = mfma16(W45[4], of2_1, pc2_5_0); SB;
        const f32x4 pc1_4_2 = mfma16(W45[2], of1_2, pc1_4_1); SB;
        const f32x4 pc2_4_1 = mfma16(W45[1], of2_1, pc2_4_0); SB;
        const f32x4 pc0_4_2 = mfma16(W45[2], of0_2, pc0_4_1); SB;
        const f32x4 pc2_4_2 = mfma16(W45[2], of2_2, pc2_4_1); SB;
        const unsigned poc0_4_0 = pk2(pc0_4_2[0], pc0_4_2[1]); SB;
        const f32x4 pc1_5_2 = mfma16(W45[5], of1_2, pc1_5_1); SB;
        const f32x4 pc2_5_2 = mfma16(W45[5], of2_2, pc2_5_1); SB;
        const unsigned poc0_4_1 = pk2(pc0_4_2[2], pc0_4_2[3]); SB;
        const unsigned poc0_5_0 = pk2(pc0_5_2[0], pc0_5_2[1]); SB;
        const unsigned poc0_5_1 = pk2(pc0_5_2[2], pc0_5_2[3]); SB;
        const unsigned poc1_4_0 = pk2(pc1_4_2[0], pc1_4_2[1]); SB;
        const unsigned poc1_4_1 = pk2(pc1_4_2[2], pc1_4_2[3]); SB;
        const unsigned poc1_5_0 = pk2(pc1_5_2[0], pc1_5_2[1]); SB;
        const unsigned poc2_4_0 = pk2(pc2_4_2[0], pc2_4_2[1]); SB;
        const unsigned poc1_5_1 = pk2(pc1_5_2[2], pc1_5_2[3]); SB;
        __builtin_amdgcn_s_setprio(3); SB;
        const unsigned poc2_4_1 = pk2(pc2_4_2[2], pc2_4_2[3]); SB;
        __builtin_amdgcn_raw_buffer_store_b128(u32x4{poc0_4_0, poc0_4_1, poc0_5_0, poc0_5_1}, psrd, orel[0], obase + 128, ST_AUX); SB;
        const unsigned poc2_5_0 = pk2(pc2_5_2[0], pc2_5_2[1]); SB;
        const unsigned poc2_5_1 = pk2(pc2_5_2[2], pc2_5_2[3]); SB;
        __builtin_amdgcn_raw_buffer_store_b128(u32x4{poc2_4_0, poc2_4_1, poc2_5_0, poc2_5_1}, psrd, orel[2], obase + 128, ST_AUX); SB;
        __builtin_amdgcn_raw_buffer_store_b128(u32x4{poc1_4_0, poc1_4_1, poc1_5_0, poc1_5_1}, psrd, orel[1], obase + 128, ST_AUX); SB;
        __builtin_amdgcn_s_setprio(2); SB;
        }
        dma_done();
        if constexpr (V == 1) asm volatile("" :: "v"(xp[0]), "v"(xp[1]), "v"(xp[2]), "v"(xp[3]), "v"(xp[4]), "v"(xp[5]), "v"(XLn[0]), "v"(XLn[1]), "v"(XLn[2]));
        if constexpr (V == 0) {
#pragma unroll
          for (int k = 0; k < 3; ++k) XLc[k] = XLn[k];
          rol ^= (unsigned)(OL ^ (OL + 3072));
        }
        if constexpr (V == 2) hlw ^= (unsigned)(HL0 ^ HL1);
        obase += ocx >= NWS - PERX ? STEP_WRAP : STEP_IN;
        ocx += ocx >= NWS - PERX ? PERX - NWS : PERX;
        asm volatile("" : "+s"(obase), "+s"(ocx));
        STAMP(20 + V);
        bar_vm<CBAR[V]>();
        STAMP(24 + V);
      };
      unsigned oL0, oL1;
        {
          auto orel_of = [&](int m0) {
            const int m = m0 + t;
            const unsigned wi = (unsigned)(wstart + m * PERX), cy = wi / (unsigned)NWS;
            const unsigned o = wi * (unsigned)(7 * PIXB) + cy * (unsigned)((7 * IMG - 7 * NWS) * PIXB) + (unsigned)(6 * IMG * PIXB) + g * 16;
            return ((t < 4) & (m < nwin)) ? o : OOB;
          };
          oL0 = orel_of(16);
          oL1 = orel_of(20);
          asm volatile("" : "+v"(oL0), "+v"(oL1));
        }
      if constexpr (DDM & 1) {
        dma_tile(IC<0>{}, ldsbase + 3 * 16384);
        dma_part(IC<0>{}, IC<12>{}, IC<3>{}, ldsbase);
        dma_done();
      } else {
#pragma nounroll
        for (int m = 3; m < 5; ++m) dma_tile(IC<0>{}, ldsbase + (m & 3) * 16384);
      }
      if constexpr (EARLY1 > 0) dma_part(IC<1>{}, IC<0>{}, IC<EARLY1>{}, ldsbase);
#pragma unroll
      for (int k = 0; k < 3; ++k) XLc[k] = XLn[k];
      rol ^= (unsigned)(OL ^ (OL + 3072));
      obase += ocx >= NWS - PERX ? STEP_WRAP : STEP_IN;
      ocx += ocx >= NWS - PERX ? PERX - NWS : PERX;
      STAMP(20);
      bar_vm<((DDM & 1) ? 3 : 15) + EARLY1>();
      STAMP(24);
#pragma nounroll
      for (int n = 1; n < 21; n += 4) {
        stepC(IC<1>{});
        if (n + 3 < nwin) stepC(IC<2>{});
        if (n + 4 < nwin) { stepC(IC<3>{}); stepC(IC<0>{}); }
      }
      {
        const __amdgpu_buffer_rsrc_t psrd = osrd;
        __builtin_amdgcn_s_setprio(TPC);
        auto mainC = [&](auto NC) {
          constexpr int N = decltype(NC)::value;
          constexpr int OOFF = ((N + 1) & 1) * 9216, QOFF = ((N + 1) & 1) * 3072;
          SB;
        const u32x4 of0_0 = lds128(smem, ro + OOFF + 0); SB;
        const u32x4 of0_1 = lds128(smem, ro + OOFF + 1024); SB;
        const u32x4 of0_2 = lds128(smem, ro + OOFF + 2048); SB;
        const u32x4 xq0 = lds128(smem, rq + QOFF + 0); SB;
        const u32x4 of1_0 = lds128(smem, ro + OOFF + 3072); SB;
        const u32x4 of1_1 = lds128(smem, ro + OOFF + 4096); SB;
        const u32x4 of1_2 = lds128(smem, ro + OOFF + 5120); SB;
        const u32x4 xq1 = lds128(smem, rq + QOFF + 1024); SB;
        const u32x4 of2_0 = lds128(smem, ro + OOFF + 6144); SB;
        const u32x4 of2_1 = lds128(smem, ro + OOFF + 7168); SB;
        const u32x4 of2_2 = lds128(smem, ro + OOFF + 8192); SB;
        const u32x4 xq2 = lds128(smem, rq + QOFF + 2048); SB;
        const f32x4 pic0_4 = mfma16(SEL0, xq0, ZERO4); SB;
        const f32x4 pic0_5 = mfma16(SEL1, xq0, ZERO4); SB;
        const f32x4 pc0_4_0 = mfma16(W45[0], of0_0, pic0_4); SB;
        const f32x4 pc0_5_0 = mfma16(W45[3], of0_0, pic0_5); SB;
        const f32x4 pc0_4_1 = mfma16(W45[1], of0_1, pc0_4_0); SB;
        const f32x4 pc0_5_1 = mfma16(W45[4], of0_1, pc0_5_0); SB;
        const f32x4 pc0_4_2 = mfma16(W45[2], of0_2, pc0_4_1); SB;
        const f32x4 pc0_5_2 = mfma16(W45[5], of0_2, pc0_5_1); SB;
        const f32x4 pic1_4 = mfma16(SEL0, xq1, ZERO4); SB;
        const f32x4 pic1_5 = mfma16(SEL1, xq1, ZERO4); SB;
        const unsigned poc0_4_0 = pk2(pc0_4_2[0], pc0_4_2[1]); SB;
        const f32x4 pc1_4_0 = mfma16(W45[0], of1_0, pic1_4); SB;
        const unsigned poc0_4_1 = pk2(pc0_4_2[2], pc0_4_2[3]); SB;
        const f32x4 pc1_5_0 = mfma16(W45[3], of1_0, pic1_5); SB;
        const unsigned poc0_5_0 = pk2(pc0_5_2[0], pc0_5_2[1]); SB;
        const f32x4 pc1_4_1 = mfma16(W45[1], of1_1, pc1_4_0); SB;
        const unsigned poc0_5_1 = pk2(pc0_5_2[2], pc0_5_2[3]); SB;
        const f32x4 pc1_5_1 = mfma16(W45[4], of1_1, pc1_5_0); SB;
        __builtin_amdgcn_raw_buffer_store_b128(u32x4{poc0_4_0, poc0_4_1, poc0_5_0, poc0_5_1}, psrd, orel[0], obase + 128, ST_AUX); SB;
        const f32x4 pc1_4_2 = mfma16(W45[2], of1_2, pc1_4_1); SB;
        const f32x4 pc1_5_2 = mfma16(W45[5], of1_2, pc1_5_1); SB;
        const f32x4 pic2_4 = mfma16(SEL0, xq2, ZERO4); SB;
        const f32x4 pic2_5 = mfma16(SEL1, xq2, ZERO4); SB;
        const unsigned poc1_4_0 = pk2(pc1_4_2[0], pc1_4_2[1]); SB;
        const f32x4 pc2_4_0 = mfma16(W45[0], of2_0, pic2_4); SB;
        const unsigned poc1_4_1 = pk2(pc1_4_2[2], pc1_4_2[3]); SB;
        const f32x4 pc2_5_0 = mfma16(W45[3], of2_0, pic2_5); SB;
        const unsigned poc1_5_0 = pk2(pc1_5_2[0], pc1_5_2[1]); SB;
        const f32x4 pc2_4_1 = mfma16(W45[1], of2_1, pc2_4_0); SB;
        const unsigned poc1_5_1 = pk2(pc1_5_2[2], pc1_5_2[3]); SB;
        const f32x4 pc2_5_1 = mfma16(W45[4], of2_1, pc2_5_0); SB;
        __builtin_amdgcn_raw_buffer_store_b128(u32x4{poc1_4_0, poc1_4_1, poc1_5_0, poc1_5_1}, psrd, orel[1], obase + 128, ST_AUX); SB;
        const f32x4 pc2_4_2 = mfma16(W45[2], of2_2, pc2_4_1); SB;
        const f32x4 pc2_5_2 = mfma16(W45[5], of2_2, pc2_5_1); SB;
        const unsigned poc2_4_0 = pk2(pc2_4_2[0], pc2_4_2[1]); SB;
        const unsigned poc2_4_1 = pk2(pc2_4_2[2], pc2_4_2[3]); SB;
        const unsigned poc2_5_0 = pk2(pc2_5_2[0], pc2_5_2[1]); SB;
        const unsigned poc2_5_1 = pk2(pc2_5_2[2], pc2_5_2[3]); SB;
        __builtin_amdgcn_raw_buffer_store_b128(u32x4{poc2_4_0, poc2_4_1, poc2_5_0, poc2_5_1}, psrd, orel[2], obase + 128, ST_AUX); SB;
          if (N == nwin - 2) {
            const unsigned xf = xfree_of(nwin);
            *(u32x4*)(smem + xf + l16) = XLc[0]; *(u32x4*)(smem + xf + 1024 + l16) = XLc[1];
            *(u32x4*)(smem + xf + 2048 + l16) = XLn[0]; *(u32x4*)(smem + xf + 3072 + l16) = XLn[1];
            *(unsigned*)(smem + xf + 4096 + lane * 4) = oL0; *(unsigned*)(smem + xf + 4096 + 256 + lane * 4) = oL1;
          }
        };
        auto drainC = [&](auto NC, const u32x4* XLc, unsigned rol, unsigned orelL) {
          constexpr int N = decltype(NC)::value;
          constexpr int OOFF = ((N + 1) & 1) * 9216, QOFF = ((N + 1) & 1) * 3072;
          SB;
        const u32x4 ol0 = lds128(smem, rol + 0); SB;
        const u32x4 ol1 = lds128(smem, rol + 1024); SB;
        const u32x4 ol2 = lds128(smem, rol + 2048); SB;
        const u32x4 of0_0 = lds128(smem, ro + OOFF + 0); SB;
        const u32x4 of0_1 = lds128(smem, ro + OOFF + 1024); SB;
        const u32x4 of0_2 = lds128(smem, ro + OOFF + 2048); SB;
        const u32x4 xq0 = lds128(smem, rq + QOFF + 0); SB;
        const u32x4 of1_0 = lds128(smem, ro + OOFF + 3072); SB;
        const u32x4 of1_1 = lds128(smem, ro + OOFF + 4096); SB;
        const u32x4 of1_2 = lds128(smem, ro + OOFF + 5120); SB;
        const u32x4 xq1 = lds128(smem, rq + QOFF + 1024); SB;
        const u32x4 of2_0 = lds128(smem, ro + OOFF + 6144); SB;
        const u32x4 of2_1 = lds128(smem, ro + OOFF + 7168); SB;
        const u32x4 of2_2 = lds128(smem, ro + OOFF + 8192); SB;
        const u32x4 xq2 = lds128(smem, rq + QOFF + 2048); SB;
        const f32x4 pil4 = mfma16(SEL0, XLc[2], ZERO4); SB;
        const f32x4 pil5 = mfma16(SEL1, XLc[2], ZERO4); SB;
        const f32x4 pl4_0 = mfma16(W45[0], ol0, pil4); SB;
        const f32x4 pl5_0 = mfma16(W45[3], ol0, pil5); SB;
        const f32x4 pl4_1 = mfma16(W45[1], ol1, pl4_0); SB;
        const f32x4 pl5_1 = mfma16(W45[4], ol1, pl5_0); SB;
        const f32x4 pl4_2 = mfma16(W45[2], ol2, pl4_1); SB;
        const f32x4 pl5_2 = mfma16(W45[5], ol2, pl5_1); SB;
        const f32x4 pic0_4 = mfma16(SEL0, xq0, ZERO4); SB;
        const f32x4 pic0_5 = mfma16(SEL1, xq0, ZERO4); SB;
        const unsigned pol4_0 = pk2(pl4_2[0], pl4_2[1]); SB;
        const f32x4 pc0_4_0 = mfma16(W45[0], of0_0, pic0_4); SB;
        const unsigned pol4_1 = pk2(pl4_2[2], pl4_2[3]); SB;
        const f32x4 pc0_5_0 = mfma16(W45[3], of0_0, pic0_5); SB;
        const unsigned pol5_0 = pk2(pl5_2[0], pl5_2[1]); SB;
        const f32x4 pc0_4_1 = mfma16(W45[1], of0_1, pc0_4_0); SB;
        const unsigned pol5_1 = pk2(pl5_2[2], pl5_2[3]); SB;
        const f32x4 pc0_5_1 = mfma16(W45[4], of0_1, pc0_5_0); SB;
        __builtin_amdgcn_raw_buffer_store_b128(u32x4{pol4_0, pol4_1, pol5_0, pol5_1}, osrd, orelL, 128, ST_AUX); SB;
        const f32x4 pc0_4_2 = mfma16(W45[2], of0_2, pc0_4_1); SB;
        const f32x4 pc0_5_2 = mfma16(W45[5], of0_2, pc0_5_1); SB;
        const f32x4 pic1_4 = mfma16(SEL0, xq1, ZERO4); SB;
        const f32x4 pic1_5 = mfma16(SEL1, xq1, ZERO4); SB;
        const unsigned poc0_4_0 = pk2(pc0_4_2[0], pc0_4_2[1]); SB;
        const f32x4 pc1_4_0 = mfma16(W45[0], of1_0, pic1_4); SB;
        const unsigned poc0_4_1 = pk2(pc0_4_2[2], pc0_4_2[3]); SB;
        const f32x4 pc1_5_0 = mfma16(W45[3], of1_0, pic1_5); SB;
        const unsigned poc0_5_0 = pk2(pc0_5_2[0], pc0_5_2[1]); SB;
        const f32x4 pc1_4_1 = mfma16(W45[1], of1_1, pc1_4_0); SB;
        const unsigned poc0_5_1 = pk2(pc0_5_2[2], pc0_5_2[3]); SB;
        const f32x4 pc1_5_1 = mfma16(W45[4], of1_1, pc1_5_0); SB;
        __builtin_amdgcn_raw_buffer_store_b128(u32x4{poc0_4_0, poc0_4_1, poc0_5_0, poc0_5_1}, psrd, orel[0], obase + 128, ST_AUX); SB;
        const f32x4 pc1_4_2 = mfma16(W45[2], of1_2, pc1_4_1); SB;
        const f32x4 pc1_5_2 = mfma16(W45[5], of1_2, pc1_5_1); SB;
        const f32x4 pic2_4 = mfma16(SEL0, xq2, ZERO4); SB;
        const f32x4 pic2_5 = mfma16(SEL1, xq2, ZERO4); SB;
        const unsigned poc1_4_0 = pk2(pc1_4_2[0], pc1_4_2[1]); SB;
        const f32x4 pc2_4_0 = mfma16(W45[0], of2_0, pic2_4); SB;
        const unsigned poc1_4_1 = pk2(pc1_4_2[2], pc1_4_2[3]); SB;
        const f32x4 pc2_5_0 = mfma16(W45[3], of2_0, pic2_5); SB;
        const unsigned poc1_5_0 = pk2(pc1_5_2[0], pc1_5_2[1]); SB;
        const f32x4 pc2_4_1 = mfma16(W45[1], of2_1, pc2_4_0); SB;
        const unsigned poc1_5_1 = pk2(pc1_5_2[2], pc1_5_2[3]); SB;
        const f32x4 pc2_5_1 = mfma16(W45[4], of2_1, pc2_5_0); SB;
        __builtin_amdgcn_raw_buffer_store_b128(u32x4{poc1_4_0, poc1_4_1, poc1_5_0, poc1_5_1}, psrd, orel[1], obase + 128, ST_AUX); SB;
        const f32x4 pc2_4_2 = mfma16(W45[2], of2_2, pc2_4_1); SB;
        const f32x4 pc2_5_2 = mfma16(W45[5], of2_2, pc2_5_1); SB;
        const unsigned poc2_4_0 = pk2(pc2_4_2[0], pc2_4_2[1]); SB;
        const unsigned poc2_4_1 = pk2(pc2_4_2[2], pc2_4_2[3]); SB;
        const unsigned poc2_5_0 = pk2(pc2_5_2[0], pc2_5_2[1]); SB;
        const unsigned poc2_5_1 = pk2(pc2_5_2[2], pc2_5_2[3]); SB;
        __builtin_amdgcn_raw_buffer_store_b128(u32x4{poc2_4_0, poc2_4_1, poc2_5_0, poc2_5_1}, psrd, orel[2], obase + 128, ST_AUX); SB;
        };
        auto endC = [&](auto NC) {
          obase += ocx >= NWS - PERX ? STEP_WRAP : STEP_IN;
          ocx += ocx >= NWS - PERX ? PERX - NWS : PERX;
          asm volatile("" : "+s"(obase), "+s"(ocx));
          BAR_LGKM();
        };
        if (!(nwin & 1)) { mainC(IC<18>{}); endC(IC<18>{}); }
        mainC(IC<19>{}); endC(IC<19>{});
        drainC(IC<20>{}, XLc, rol, oL0);
        if (nwin & 1) { endC(IC<20>{}); drainC(IC<21>{}, XLn, rol ^ (unsigned)(OL ^ (OL + 3072)), oL1); }
      }
      TSLOUT();
      STAMP(10);
    }
  } else {
    f32x4 cV1, cS3, cLD;
    const bool g0 = g == 0, g1 = g == 1, g2 = g == 2;
    unsigned hb, hl, ow, owl, psw, psr;
    u32x4 Qf0, Qf1, Qf2, Qf3, Kf0, Kf1, Kf2, Kf3, PB0, PB1, PB2, VA0, VA1, VB0, VB1;
#define ZINIT(X) do { const unsigned z_ = hb & szero(); X = u32x4{z_, z_, z_, z_}; } while (0)
    u32x4 WQ[3], WK[3], WX[3], WV0[3], WV1[3];
    bool force_safe;
    {
      int aQ[6], aX[6], aV1[6];
      Cur c1 = cur_at(1);
      asm volatile("" : "+s"(c1.base), "+s"(c1.cx));
      u32x4 wsrd;
      wsrd[0] = (unsigned)(size_t)wqkv; wsrd[1] = (unsigned)((size_t)wqkv >> 32) & 0xffffu; wsrd[2] = 55296u; wsrd[3] = 0x00020000u;
      TS(2);
      const unsigned wdst = ldsbase + w * 14336;
      auto wpiece = [&](int q) { dma16<RAWQ>(wdst + q * 1024, wvo + q * 4032u, wsrd, 0u); SB; };
#pragma unroll
      for (int q = 0; q < 4; ++q) { wpiece(q); make_rel1(q); SB; }
#pragma unroll
      for (int u = 0; u < 6; ++u) {
        wpiece(4 + u);
        const int cq = t & 3;
        const int cQ = 8 * cq;
        const int cX = cq < 2 ? 32 + 8 * cq : 48 + 32 + 8 * (cq - 2);
        const int cV1 = 96 + 32 + 8 * (cq >> 1);
        const unsigned row = 16 * u + 4 * g + (t >> 2);
        const int ro = RAWQ + w * 14336 + __umul24(row, 144u) + (__umul24(row, 147u) >> 10) * 16u;
        aQ[u] = ro + cQ; aX[u] = ro + cX; aV1[u] = ro + cV1;
        asm volatile("" : "+v"(aQ[u]), "+v"(aX[u]), "+v"(aV1[u]));
        SB;
      }
      wpiece(10);
      {
        float o = (t & 3) == 2 ? 1.0f : 0.0f;
        cV1 = f32x4{o, o, o, o};
        float m = g == 0 ? 0.0f : -INFINITY;
        cS3 = f32x4{m, m, m, m};
        cLD = f32x4{t == 0 ? 0.0f : -INFINITY, t == 1 ? 0.0f : -INFINITY, t == 2 ? 0.0f : -INFINITY, t == 3 ? 0.0f : -INFINITY};
        asm volatile("" : "+v"(cS3), "+v"(cV1), "+v"(cLD));
        SB;
      }
      wpiece(11);
      hb = H1 + l16;
      hl = HL0 + l16;
      ow = OR_ + w * 256 + t * 16 + g * 4;
      owl = OL + w * 256 + (t & 12) * 16 + g * 4;
      psw = PS + w * 128 + (g >> 1) * 64 + (t >> 2) * 16 + ((g & 1) * 4 + (t & 3)) * 2;
      psr = PS + w * 128 + g * 16;
      asm volatile("" : "+v"(hb), "+v"(hl), "+v"(ow), "+v"(owl), "+v"(psw), "+v"(psr));
      SB;
      wpiece(12);
      ZINIT(Qf0); ZINIT(Qf1); ZINIT(Qf2); ZINIT(Qf3); ZINIT(Kf0); ZINIT(Kf1); ZINIT(Kf2); ZINIT(Kf3);
      SB;
      wpiece(13);
      STAMP(13);
      TS(3);
      asm volatile("s_barrier" ::: "memory");
      tile4(IC<1>{}, c1);
      ZINIT(PB0); ZINIT(PB1); ZINIT(PB2); ZINIT(VA0); ZINIT(VA1); ZINIT(VB0); ZINIT(VB1);
      SB;
      STAMP(15);
      TS(4);
      WAIT_VM(4);
      TS(5);
      STAMP(8);
      const bool odd = (t >> 2) & 1;
      const bool keep = (t & 3) < 2;
      u32x2 rv1[6];
#pragma unroll
      for (int u = 0; u < 6; ++u) rv1[u] = trread(smem, aV1[u]);
#pragma unroll
      for (int u = 0; u < 6; ++u) {
        const u32x2 v0 = trread(smem, aQ[u] + 96), kk = trread(smem, aQ[u] + 48), q = trread(smem, aQ[u]), xx = trread(smem, aX[u]);
        const int k = u >> 1, j = 2 * (u & 1);
        WV0[k][j] = v0[0]; WV0[k][j + 1] = v0[1];
        WK[k][j] = kk[0]; WK[k][j + 1] = kk[1];
        WQ[k][j] = q[0]; WQ[k][j + 1] = q[1];
        WX[k][j] = xx[0]; WX[k][j + 1] = xx[1];
      }
      SB;
#pragma unroll
      for (int u = 0; u < 6; ++u) {
#pragma unroll
        for (int d = 0; d < 2; ++d) {
          const unsigned v1 = rv1[u][d];
          const unsigned sh = __builtin_amdgcn_update_dpp(0u, v1, 0xEE, 0xF, 0xF, true);
          const unsigned vv = odd ? sh : v1;
          WV1[u >> 1][2 * (u & 1) + d] = keep ? vv : 0u;
        }
      }
      f32x4 ss = ZERO4;
#pragma unroll
      for (int k = 0; k < 3; ++k) { ss = mfma16(WV0[k], WV0[k], ss); ss = mfma16(WV1[k], WV1[k], ss); }
      const bool big = !(fabsf(ss[0]) < 1.0e36f) | !(fabsf(ss[1]) < 1.0e36f) | !(fabsf(ss[2]) < 1.0e36f) | !(fabsf(ss[3]) < 1.0e36f);
      force_safe = __builtin_amdgcn_ballot_w64(big) != 0ull;
    }
    const unsigned safe_thr = force_safe ? 0u : 0x53800000u;
    STAMP(9);
    TS(6);
    BAR_VM(0);
    __builtin_amdgcn_s_setprio(P2_B);
    TS(7);
    STAMP(5);
    {
      constexpr int HOFF = hslot(0);
      const float qs = 0.20412414523193154f * 1.4426950408889634f;
      const float sx = t < 8 ? qs : 1.0f;
        const u32x4 hf0_0 = lds128(smem, hb + HOFF + 0); SB;
        const u32x4 hf0_1 = lds128(smem, hb + HOFF + 1024); SB;
        const float wxx0_0l = bf_lo(WX[0][0]); SB;
        const float wxx0_1h = bf_hi(WX[0][1]); SB;
        const float wxx0_2h = bf_hi(WX[0][2]); SB;
        const float wxx0_2b = wxx0_2h * sx; SB;
        const float wxx0_0a = wxx0_0l * sx; SB;
        const float wxx0_1l = bf_lo(WX[0][1]); SB;
        const float wxx0_1a = wxx0_1l * sx; SB;
        const float wxx0_3h = bf_hi(WX[0][3]); SB;
        const float wq0_3h = bf_hi(WQ[0][3]); SB;
        const float wxx0_1b = wxx0_1h * sx; SB;
        WX[0][1] = pk2(wxx0_1a, wxx0_1b); SB;
        const float wxx0_0h = bf_hi(WX[0][0]); SB;
        const float wxx0_0b = wxx0_0h * sx; SB;
        WX[0][0] = pk2(wxx0_0a, wxx0_0b); SB;
        const float wxx1_2h = bf_hi(WX[1][2]); SB;
        const float wxx0_3l = bf_lo(WX[0][3]); SB;
        const float wxx0_3a = wxx0_3l * sx; SB;
        const float wq0_3b = wq0_3h * qs; SB;
        const float wxx1_2b = wxx1_2h * sx; SB;
        const float wq0_1l = bf_lo(WQ[0][1]); SB;
        const float wq0_1a = wq0_1l * qs; SB;
        const float wq1_0l = bf_lo(WQ[1][0]); SB;
        const float wq0_0l = bf_lo(WQ[0][0]); SB;
        const float wq0_0a = wq0_0l * qs; SB;
        const float wxx0_2l = bf_lo(WX[0][2]); SB;
        const float wxx0_2a = wxx0_2l * sx; SB;
        WX[0][2] = pk2(wxx0_2a, wxx0_2b); SB;
        const float wxx1_2l = bf_lo(WX[1][2]); SB;
        const float wxx1_2a = wxx1_2l * sx; SB;
        const float wq0_3l = bf_lo(WQ[0][3]); SB;
        const float wq0_3a = wq0_3l * qs; SB;
        const float wq0_0h = bf_hi(WQ[0][0]); SB;
        const float wxx1_3l = bf_lo(WX[1][3]); SB;
        const float wxx1_3a = wxx1_3l * sx; SB;
        const float wxx0_3b = wxx0_3h * sx; SB;
        WX[0][3] = pk2(wxx0_3a, wxx0_3b); SB;
        WQ[0][3] = pk2(wq0_3a, wq0_3b); SB;
        const float wxx1_1h = bf_hi(WX[1][1]); SB;
        const float wq1_3h = bf_hi(WQ[1][3]); SB;
        const float wxx1_3h = bf_hi(WX[1][3]); SB;
        const float wxx1_3b = wxx1_3h * sx; SB;
        const float wq0_2h = bf_hi(WQ[0][2]); SB;
        const float wq0_2b = wq0_2h * qs; SB;
        const float wq1_0a = wq1_0l * qs; SB;
        const float wq0_2l = bf_lo(WQ[0][2]); SB;
        const float wq0_2a = wq0_2l * qs; SB;
        const float wq1_2l = bf_lo(WQ[1][2]); SB;
        const float wq0_1h = bf_hi(WQ[0][1]); SB;
        const float wxx2_2l = bf_lo(WX[2][2]); SB;
        const float wxx1_1l = bf_lo(WX[1][1]); SB;
        const float wxx1_1a = wxx1_1l * sx; SB;
        const float wxx2_3h = bf_hi(WX[2][3]); SB;
        const float wq0_0b = wq0_0h * qs; SB;
        const u32x4 hf0_2 = lds128(smem, hb + HOFF + 2048); SB;
        const f32x4 xx0_0 = mfma16(WX[0], hf0_0, ZERO4); SB;
        WQ[0][0] = pk2(wq0_0a, wq0_0b); SB;
        const float wxx1_0l = bf_lo(WX[1][0]); SB;
        const float wq0_1b = wq0_1h * qs; SB;
        WQ[0][1] = pk2(wq0_1a, wq0_1b); SB;
        WQ[0][2] = pk2(wq0_2a, wq0_2b); SB;
        const float wxx1_0h = bf_hi(WX[1][0]); SB;
        const float wxx1_0b = wxx1_0h * sx; SB;
        const float wq1_2a = wq1_2l * qs; SB;
        const f32x4 v10_0 = mfma16(hf0_0, WV1[0], cV1); SB;
        const float wq1_2h = bf_hi(WQ[1][2]); SB;
        const float wxx2_2h = bf_hi(WX[2][2]); SB;
        const float wxx2_3b = wxx2_3h * sx; SB;
        const float wq1_1h = bf_hi(WQ[1][1]); SB;
        const f32x4 q0_0 = mfma16(WQ[0], hf0_0, ZERO4); SB;
        WX[1][2] = pk2(wxx1_2a, wxx1_2b); SB;
        const float wxx1_1b = wxx1_1h * sx; SB;
        WX[1][1] = pk2(wxx1_1a, wxx1_1b); SB;
        const f32x4 v00_0 = mfma16(hf0_0, WV0[0], ZERO4); SB;
        const u32x4 hf1_0 = lds128(smem, hb + HOFF + 3072); SB;
        const f32x4 kk0_0 = mfma16(WK[0], hf0_0, ZERO4); SB;
        const u32x4 hf1_1 = lds128(smem, hb + HOFF + 4096); SB;
        const float wq2_2l = bf_lo(WQ[2][2]); SB;
        const u32x4 hf1_2 = lds128(smem, hb + HOFF + 5120); SB;
        const float wq1_3l = bf_lo(WQ[1][3]); SB;
        const u32x4 hf2_0 = lds128(smem, hb + HOFF + 6144); SB;
        const float wq1_3a = wq1_3l * qs; SB;
        const u32x4 hf2_1 = lds128(smem, hb + HOFF + 7168); SB;
        const float wxx1_0a = wxx1_0l * sx; SB;
        const u32x4 hf2_2 = lds128(smem, hb + HOFF + 8192); SB;
        const float wq1_3b = wq1_3h * qs; SB;
        const u32x4 hf3_0 = lds128(smem, hl + 0); SB;
        WQ[1][3] = pk2(wq1_3a, wq1_3b); SB;
        const u32x4 hf3_1 = lds128(smem, hl + 1024); SB;
        WX[1][3] = pk2(wxx1_3a, wxx1_3b); SB;
        const u32x4 hf3_2 = lds128(smem, hl + 2048); SB;
        WX[1][0] = pk2(wxx1_0a, wxx1_0b); SB;
        const float wq1_1l = bf_lo(WQ[1][1]); SB;
        const float wq1_1a = wq1_1l * qs; SB;
        const f32x4 kk1_0 = mfma16(WK[0], hf1_0, ZERO4); SB;
        const float wxx2_2b = wxx2_2h * sx; SB;
        const float wq1_0h = bf_hi(WQ[1][0]); SB;
        const float wq1_0b = wq1_0h * qs; SB;
        const float wq1_1b = wq1_1h * qs; SB;
        WQ[1][1] = pk2(wq1_1a, wq1_1b); SB;
        const float wxx2_2a = wxx2_2l * sx; SB;
        const f32x4 v11_0 = mfma16(hf1_0, WV1[0], cV1); SB;
        WX[2][2] = pk2(wxx2_2a, wxx2_2b); SB;
        const float wxx2_1l = bf_lo(WX[2][1]); SB;
        const float wxx2_1a = wxx2_1l * sx; SB;
        const float wxx2_0h = bf_hi(WX[2][0]); SB;
        const float wxx2_0b = wxx2_0h * sx; SB;
        const float wq1_2b = wq1_2h * qs; SB;
        const float wq2_1l = bf_lo(WQ[2][1]); SB;
        const float wq2_1a = wq2_1l * qs; SB;
        const f32x4 xx1_0 = mfma16(WX[0], hf1_0, ZERO4); SB;
        const float wxx2_0l = bf_lo(WX[2][0]); SB;
        const f32x4 xx3_0 = mfma16(WX[0], hf3_0, ZERO4); SB;
        const f32x4 q3_0 = mfma16(WQ[0], hf3_0, ZERO4); SB;
        const float wq2_1h = bf_hi(WQ[2][1]); SB;
        WQ[1][0] = pk2(wq1_0a, wq1_0b); SB;
        const f32x4 v13_0 = mfma16(hf3_0, WV1[0], cV1); SB;
        const float wq2_1b = wq2_1h * qs; SB;
        const float wq2_3l = bf_lo(WQ[2][3]); SB;
        const f32x4 xx0_1 = mfma16(WX[1], hf0_1, xx0_0); SB;
        const float wq2_0l = bf_lo(WQ[2][0]); SB;
        const float wq2_0a = wq2_0l * qs; SB;
        const f32x4 v00_1 = mfma16(hf0_1, WV0[1], v00_0); SB;
        const float wq2_3a = wq2_3l * qs; SB;
        const float wxx2_0a = wxx2_0l * sx; SB;
        const f32x4 v11_1 = mfma16(hf1_1, WV1[1], v11_0); SB;
        WX[2][0] = pk2(wxx2_0a, wxx2_0b); SB;
        WQ[1][2] = pk2(wq1_2a, wq1_2b); SB;
        const f32x4 kk1_1 = mfma16(WK[1], hf1_1, kk1_0); SB;
        const float wxx2_3l = bf_lo(WX[2][3]); SB;
        const float wxx2_3a = wxx2_3l * sx; SB;
        const f32x4 xx1_1 = mfma16(WX[1], hf1_1, xx1_0); SB;
        WX[2][3] = pk2(wxx2_3a, wxx2_3b); SB;
        const float wxx2_1h = bf_hi(WX[2][1]); SB;
        const f32x4 v10_1 = mfma16(hf0_1, WV1[1], v10_0); SB;
        const float wxx2_1b = wxx2_1h * sx; SB;
        WX[2][1] = pk2(wxx2_1a, wxx2_1b); SB;
        const f32x4 kk2_0 = mfma16(WK[0], hf2_0, ZERO4); SB;
        const float wq2_0h = bf_hi(WQ[2][0]); SB;
        const float wq2_0b = wq2_0h * qs; SB;
        const f32x4 kk2_1 = mfma16(WK[1], hf2_1, kk2_0); SB;
        const float wq2_2a = wq2_2l * qs; SB;
        const float wq2_2h = bf_hi(WQ[2][2]); SB;
        const f32x4 kk0_1 = mfma16(WK[1], hf0_1, kk0_0); SB;
        const float wq2_2b = wq2_2h * qs; SB;
        const float wq2_3h = bf_hi(WQ[2][3]); SB;
        const f32x4 q0_1 = mfma16(WQ[1], hf0_1, q0_0); SB;
        const float wq2_3b = wq2_3h * qs; SB;
        WQ[2][3] = pk2(wq2_3a, wq2_3b); SB;
        const f32x4 xx1_2 = mfma16(WX[2], hf1_2, xx1_1); SB;
        WQ[2][1] = pk2(wq2_1a, wq2_1b); SB;
        WQ[2][0] = pk2(wq2_0a, wq2_0b); SB;
        const f32x4 q2_0 = mfma16(WQ[0], hf2_0, ZERO4); SB;
        WQ[2][2] = pk2(wq2_2a, wq2_2b); SB;
        const f32x4 xx2_0 = mfma16(WX[0], hf2_0, ZERO4); SB;
        const f32x4 q2_1 = mfma16(WQ[1], hf2_1, q2_0); SB;
        const unsigned xa1 = pk2(xx1_2[0], xx1_2[1]); SB;
        const f32x4 xx2_1 = mfma16(WX[1], hf2_1, xx2_0); SB;
        const unsigned xc1 = pk2(xx1_2[2], xx1_2[3]); SB;
        const f32x4 v03_0 = mfma16(hf3_0, WV0[0], ZERO4); SB;
        const auto sw1 = __builtin_amdgcn_permlane32_swap(xa1, xc1, false, false); SB;
        const f32x4 v00_2 = mfma16(hf0_2, WV0[2], v00_1); SB;
        const f32x4 q3_1 = mfma16(WQ[1], hf3_1, q3_0); SB;
        const f32x4 v13_1 = mfma16(hf3_1, WV1[1], v13_0); SB;
        const f32x4 xx3_1 = mfma16(WX[1], hf3_1, xx3_0); SB;
        const f32x4 xx3_2 = mfma16(WX[2], hf3_2, xx3_1); SB;
        VA0[0] = pk2(v00_2[0], v00_2[1]); SB;
        const f32x4 v03_1 = mfma16(hf3_1, WV0[1], v03_0); SB;
        VA0[1] = pk2(v00_2[2], v00_2[3]); SB;
        const f32x4 q1_0 = mfma16(WQ[0], hf1_0, ZERO4); SB;
        const unsigned xa3 = pk2(xx3_2[0], xx3_2[1]); SB;
        const f32x4 q1_1 = mfma16(WQ[1], hf1_1, q1_0); SB;
        const unsigned xc3 = pk2(xx3_2[2], xx3_2[3]); SB;
        const f32x4 v02_0 = mfma16(hf2_0, WV0[0], ZERO4); SB;
        const auto sw3 = __builtin_amdgcn_permlane32_swap(xa3, xc3, false, false); SB;
        const f32x4 v02_1 = mfma16(hf2_1, WV0[1], v02_0); SB;
        const f32x4 q2_2 = mfma16(WQ[2], hf2_2, q2_1); SB;
        const f32x4 v11_2 = mfma16(hf1_2, WV1[2], v11_1); SB;
        const f32x4 v02_2 = mfma16(hf2_2, WV0[2], v02_1); SB;
        const f32x4 xx2_2 = mfma16(WX[2], hf2_2, xx2_1); SB;
        const f32x4 xx0_2 = mfma16(WX[2], hf0_2, xx0_1); SB;
        const f32x4 v13_2 = mfma16(hf3_2, WV1[2], v13_1); SB;
        const f32x4 q0_2 = mfma16(WQ[2], hf0_2, q0_1); SB;
        const unsigned qf2_1 = pk2(q2_2[2], q2_2[3]); SB;
        const f32x4 q3_2 = mfma16(WQ[2], hf3_2, q3_1); SB;
        VB0[3] = pk2(v11_2[2], v11_2[3]); SB;
        const unsigned xa2 = pk2(xx2_2[0], xx2_2[1]); SB;
        const f32x4 q1_2 = mfma16(WQ[2], hf1_2, q1_1); SB;
        const unsigned xc2 = pk2(xx2_2[2], xx2_2[3]); SB;
        const unsigned xc0 = pk2(xx0_2[2], xx0_2[3]); SB;
        const f32x4 v01_0 = mfma16(hf1_0, WV0[0], ZERO4); SB;
        const unsigned xa0 = pk2(xx0_2[0], xx0_2[1]); SB;
        const auto sw2 = __builtin_amdgcn_permlane32_swap(xa2, xc2, false, false); SB;
        const f32x4 kk3_0 = mfma16(WK[0], hf3_0, ZERO4); SB;
        const auto sw0 = __builtin_amdgcn_permlane32_swap(xa0, xc0, false, false); SB;
        const f32x4 v12_0 = mfma16(hf2_0, WV1[0], cV1); SB;
        VA1[1] = pk2(v02_2[2], v02_2[3]); SB;
        VB0[2] = pk2(v11_2[0], v11_2[1]); SB;
        const f32x4 v01_1 = mfma16(hf1_1, WV0[1], v01_0); SB;
        VB1[3] = pk2(v13_2[2], v13_2[3]); SB;
        const unsigned qf0_0 = pk2(q0_2[0], q0_2[1]); SB;
        const f32x4 v12_1 = mfma16(hf2_1, WV1[1], v12_0); SB;
        const unsigned qf3_0 = pk2(q3_2[0], q3_2[1]); SB;
        VA1[0] = pk2(v02_2[0], v02_2[1]); SB;
        const f32x4 kk3_1 = mfma16(WK[1], hf3_1, kk3_0); SB;
        const unsigned qf1_0 = pk2(q1_2[0], q1_2[1]); SB;
        const unsigned qf3_1 = pk2(q3_2[2], q3_2[3]); SB;
        Qf3[0] = qf3_0; Qf3[1] = qf3_1; Qf3[2] = sw3[0];
        const f32x4 v01_2 = mfma16(hf1_2, WV0[2], v01_1); SB;
        const unsigned qf2_0 = pk2(q2_2[0], q2_2[1]); SB;
        Qf2[0] = qf2_0; Qf2[1] = qf2_1; Qf2[2] = sw2[0];
        const unsigned qf0_1 = pk2(q0_2[2], q0_2[3]); SB;
        Qf0[0] = qf0_0; Qf0[1] = qf0_1; Qf0[2] = sw0[0];
        const f32x4 v12_2 = mfma16(hf2_2, WV1[2], v12_1); SB;
        VB1[2] = pk2(v13_2[0], v13_2[1]); SB;
        const unsigned qf1_1 = pk2(q1_2[2], q1_2[3]); SB;
        Qf1[0] = qf1_0; Qf1[1] = qf1_1; Qf1[2] = sw1[0];
        const f32x4 kk3_2 = mfma16(WK[2], hf3_2, kk3_1); SB;
        VA0[2] = pk2(v01_2[0], v01_2[1]); SB;
        const f32x4 kk1_2 = mfma16(WK[2], hf1_2, kk1_1); SB;
        VA0[3] = pk2(v01_2[2], v01_2[3]); SB;
        VB1[0] = pk2(v12_2[0], v12_2[1]); SB;
        const f32x4 kk2_2 = mfma16(WK[2], hf2_2, kk2_1); SB;
        VB1[1] = pk2(v12_2[2], v12_2[3]); SB;
        const unsigned kf3_0 = pk2(kk3_2[0], kk3_2[1]); SB;
        const f32x4 v10_2 = mfma16(hf0_2, WV1[2], v10_1); SB;
        const unsigned kf3_1 = pk2(kk3_2[2], kk3_2[3]); SB;
        Kf3[0] = kf3_0; Kf3[1] = kf3_1; Kf3[2] = sw3[1];
        const f32x4 kk0_2 = mfma16(WK[2], hf0_2, kk0_1); SB;
        const unsigned kf1_0 = pk2(kk1_2[0], kk1_2[1]); SB;
        const unsigned kf1_1 = pk2(kk1_2[2], kk1_2[3]); SB;
        Kf1[0] = kf1_0; Kf1[1] = kf1_1; Kf1[2] = sw1[1];
        const f32x4 v03_2 = mfma16(hf3_2, WV0[2], v03_1); SB;
        const unsigned kf2_1 = pk2(kk2_2[2], kk2_2[3]); SB;
        const unsigned kf2_0 = pk2(kk2_2[0], kk2_2[1]); SB;
        Kf2[0] = kf2_0; Kf2[1] = kf2_1; Kf2[2] = sw2[1];
        VB0[1] = pk2(v10_2[2], v10_2[3]); SB;
        VB0[0] = pk2(v10_2[0], v10_2[1]); SB;
        const unsigned kf0_0 = pk2(kk0_2[0], kk0_2[1]); SB;
        const unsigned kf0_1 = pk2(kk0_2[2], kk0_2[3]); SB;
        Kf0[0] = kf0_0; Kf0[1] = kf0_1; Kf0[2] = sw0[1];
        VA1[3] = pk2(v03_2[2], v03_2[3]); SB;
        VA1[2] = pk2(v03_2[0], v03_2[1]); SB;
    }
    hl ^= (unsigned)(HL0 ^ HL1);
    STAMP(6);
    TS(8);
    BAR_LGKM();
    TS(9);
    TSOUT();
    STAMP(1);
    __builtin_amdgcn_s_setprio(B_PRIO);
    auto stepB = [&](auto VC, auto TC) {
      constexpr int V = decltype(VC)::value;
      constexpr bool TAIL = decltype(TC)::value != 0;
      constexpr int HOFF = hslot((V + 1) & 3);
      constexpr int OOFF = (V & 1) * 9216;
      unsigned chk = 0;
      const unsigned zu = szero();
      const float zf = __builtin_bit_cast(float, zu);
      SB;
      if constexpr (TAIL && V == 0) {
        const f32x4 ld0 = mfma16(Qf3, Kf0, ZERO4); SB;
        PB2[3] = psw & zu; SB;
        PB0[3] = hb & zu; SB;
        const f32x4 ld3 = mfma16(Qf3, Kf3, cLD); SB;
        PB1[3] = ow & zu; SB;
        const f32x4 ld2 = mfma16(Qf3, Kf2, ZERO4); SB;
        const f32x4 ld1 = mfma16(Qf3, Kf1, ZERO4); SB;
        const f32x4 sc0_0 = mfma16(Kf0, Qf0, ZERO4); SB;
        const f32x4 sc0_1 = mfma16(Kf1, Qf0, ZERO4); SB;
        const f32x4 sc1_2 = mfma16(Kf2, Qf1, ZERO4); SB;
        const float lv0 = g0 ? ld0[0] : ld3[0]; SB;
        const f32x4 sc0_3 = mfma16(Kf3, Qf0, cS3); SB;
        const float lv1 = g1 ? ld1[0] : lv0; SB;
        const float lv2 = g2 ? ld2[0] : lv1; SB;
        const f32x4 sc1_0 = mfma16(Kf0, Qf1, ZERO4); SB;
        const float lve = __builtin_amdgcn_exp2f(lv2); SB;
        const f32x4 sc1_3 = mfma16(Kf3, Qf1, cS3); SB;
        const unsigned lvp = pk2(lve, lve); SB;
        const float e0_0_0 = __builtin_amdgcn_exp2f(sc0_0[0]); SB;
        const f32x4 sc0_2 = mfma16(Kf2, Qf0, ZERO4); SB;
        *(unsigned short*)(smem + psw) = (unsigned short)lvp; SB;
        const f32x4 sc2_0 = mfma16(Kf0, Qf2, ZERO4); SB;
        const u32x4 lp0 = lds128(smem, psr); SB;
        const u32x4 lp1 = lds128(smem, psr + 64); SB;
        const f32x4 sc1_1 = mfma16(Kf1, Qf1, ZERO4); SB;
        const float e0_1_3 = __builtin_amdgcn_exp2f(sc0_1[3]); SB;
        const f32x4 sc2_3 = mfma16(Kf3, Qf2, cS3); SB;
        const float e0_1_1 = __builtin_amdgcn_exp2f(sc0_1[1]); SB;
        const f32x4 sc2_2 = mfma16(Kf2, Qf2, ZERO4); SB;
        const float e1_2_1 = __builtin_amdgcn_exp2f(sc1_2[1]); SB;
        const f32x4 sc2_1 = mfma16(Kf1, Qf2, ZERO4); SB;
        const float e1_0_2 = __builtin_amdgcn_exp2f(sc1_0[2]); SB;
        const float e0_3 = __builtin_amdgcn_exp2f(sc0_3[0]); SB;
        const float e1_1_2 = __builtin_amdgcn_exp2f(sc1_1[2]); SB;
        const float e1_1_3 = __builtin_amdgcn_exp2f(sc1_1[3]); SB;
        const float e2_3 = __builtin_amdgcn_exp2f(sc2_3[0]); SB;
        const float e1_1_0 = __builtin_amdgcn_exp2f(sc1_1[0]); SB;
        const float e2_2_2 = __builtin_amdgcn_exp2f(sc2_2[2]); SB;
        const float e1_2_0 = __builtin_amdgcn_exp2f(sc1_2[0]); SB;
        const float e2_1_3 = __builtin_amdgcn_exp2f(sc2_1[3]); SB;
        const float e2_1_0 = __builtin_amdgcn_exp2f(sc2_1[0]); SB;
        const float e0_2_3 = __builtin_amdgcn_exp2f(sc0_2[3]); SB;
        const unsigned pp1_1_1 = pk2(e1_1_2, e1_1_3); SB;
        const float e0_0_1 = __builtin_amdgcn_exp2f(sc0_0[1]); SB;
        const float e2_0_1 = __builtin_amdgcn_exp2f(sc2_0[1]); SB;
        const unsigned pp0_0_0 = pk2(e0_0_0, e0_0_1); SB;
        const float e0_2_0 = __builtin_amdgcn_exp2f(sc0_2[0]); SB;
        const float e0_1_0 = __builtin_amdgcn_exp2f(sc0_1[0]); SB;
        const float e1_1_1 = __builtin_amdgcn_exp2f(sc1_1[1]); SB;
        const unsigned pp0_1_0 = pk2(e0_1_0, e0_1_1); SB;
        const unsigned pp1_1_0 = pk2(e1_1_0, e1_1_1); SB;
        const float e2_1_2 = __builtin_amdgcn_exp2f(sc2_1[2]); SB;
        const float e0_2_2 = __builtin_amdgcn_exp2f(sc0_2[2]); SB;
        const float e1_0_3 = __builtin_amdgcn_exp2f(sc1_0[3]); SB;
        const unsigned pp0_2_1 = pk2(e0_2_2, e0_2_3); SB;
        const unsigned pp1_0_1 = pk2(e1_0_2, e1_0_3); SB;
        const float e0_1_2 = __builtin_amdgcn_exp2f(sc0_1[2]); SB;
        const float e0_0_2 = __builtin_amdgcn_exp2f(sc0_0[2]); SB;
        const unsigned pp0_1_1 = pk2(e0_1_2, e0_1_3); SB;
        const unsigned pp1_2_0 = pk2(e1_2_0, e1_2_1); SB;
        const float e2_0_3 = __builtin_amdgcn_exp2f(sc2_0[3]); SB;
        const float e1_0_1 = __builtin_amdgcn_exp2f(sc1_0[1]); SB;
        const float e2_0_0 = __builtin_amdgcn_exp2f(sc2_0[0]); SB;
        const unsigned pp2_1_1 = pk2(e2_1_2, e2_1_3); SB;
        const unsigned pp2_0_0 = pk2(e2_0_0, e2_0_1); SB;
        const float e2_2_0 = __builtin_amdgcn_exp2f(sc2_2[0]); SB;
        const float e2_0_2 = __builtin_amdgcn_exp2f(sc2_0[2]); SB;
        const float e1_2_3 = __builtin_amdgcn_exp2f(sc1_2[3]); SB;
        const unsigned pp2_0_1 = pk2(e2_0_2, e2_0_3); SB;
        const float e1_0_0 = __builtin_amdgcn_exp2f(sc1_0[0]); SB;
        const float e2_1_1 = __builtin_amdgcn_exp2f(sc2_1[1]); SB;
        const unsigned pp1_0_0 = pk2(e1_0_0, e1_0_1); SB;
        const u32x4 PA1 = u32x4{pp1_0_0, pp1_0_1, pp1_1_0, pp1_1_1};
        const unsigned pp2_1_0 = pk2(e2_1_0, e2_1_1); SB;
        const u32x4 PA2 = u32x4{pp2_0_0, pp2_0_1, pp2_1_0, pp2_1_1};
        const float e0_0_3 = __builtin_amdgcn_exp2f(sc0_0[3]); SB;
        const f32x4 ob1 = mfma16(VB0, PA1, ZERO4); SB;
        const unsigned pp0_0_1 = pk2(e0_0_2, e0_0_3); SB;
        const u32x4 PA0 = u32x4{pp0_0_0, pp0_0_1, pp0_1_0, pp0_1_1};
        const unsigned pp0_3 = pk2(e0_3, zf); SB;
        const f32x4 oa1 = mfma16(VA0, PA1, ZERO4); SB;
        const float e0_2_1 = __builtin_amdgcn_exp2f(sc0_2[1]); SB;
        const f32x4 ob0 = mfma16(VB0, PA0, ZERO4); SB;
        const unsigned pp0_2_0 = pk2(e0_2_0, e0_2_1); SB;
        PB0[0] = pp0_2_0; PB0[1] = pp0_2_1; PB0[2] = pp0_3;
        const float e2_2_1 = __builtin_amdgcn_exp2f(sc2_2[1]); SB;
        const f32x4 ob2 = mfma16(VB0, PA2, ZERO4); SB;
        const unsigned pp2_2_0 = pk2(e2_2_0, e2_2_1); SB;
        const unsigned pp2_3 = pk2(e2_3, zf); SB;
        const f32x4 oa0 = mfma16(VA0, PA0, ZERO4); SB;
        const float e1_3 = __builtin_amdgcn_exp2f(sc1_3[0]); SB;
        const f32x4 oa2 = mfma16(VA0, PA2, ZERO4); SB;
        const unsigned pp1_3 = pk2(e1_3, zf); SB;
        const float e2_2_3 = __builtin_amdgcn_exp2f(sc2_2[3]); SB;
        const f32x4 od0 = mfma16(VB1, PB0, ob0); SB;
        const unsigned pp2_2_1 = pk2(e2_2_2, e2_2_3); SB;
        PB2[0] = pp2_2_0; PB2[1] = pp2_2_1; PB2[2] = pp2_3;
        const float e1_2_2 = __builtin_amdgcn_exp2f(sc1_2[2]); SB;
        const f32x4 oc0 = mfma16(VA1, PB0, oa0); SB;
        const unsigned pp1_2_1 = pk2(e1_2_2, e1_2_3); SB;
        PB1[0] = pp1_2_0; PB1[1] = pp1_2_1; PB1[2] = pp1_3;
        const f32x4 od2 = mfma16(VB1, PB2, ob2); SB;
        const f32x4 oa3 = mfma16(VA0, lp0, ZERO4); SB;
        const float rl0 = __builtin_amdgcn_rcpf(od0[2]); SB;
        const f32x4 od1 = mfma16(VB1, PB1, ob1); SB;
        const float om0_0 = od0[0] * rl0; SB;
        const float om0_3 = oc0[1] * rl0; SB;
        const f32x4 ob3 = mfma16(VB0, lp0, ZERO4); SB;
        const float rl2 = __builtin_amdgcn_rcpf(od2[2]); SB;
        const f32x4 oc1 = mfma16(VA1, PB1, oa1); SB;
        const float om2_1 = od2[1] * rl2; SB;
        const float om0_2 = oc0[0] * rl0; SB;
        const f32x4 oc2 = mfma16(VA1, PB2, oa2); SB;
        const float rl1 = __builtin_amdgcn_rcpf(od1[2]); SB;
        const unsigned ow0_1 = pk2(om0_2, om0_3); SB;
        const float om1_0 = od1[0] * rl1; SB;
        const float om1_1 = od1[1] * rl1; SB;
        const float om0_5 = oc0[3] * rl0; SB;
        const float om1_3 = oc1[1] * rl1; SB;
        const float om1_5 = oc1[3] * rl1; SB;
        const unsigned ow1_0 = pk2(om1_0, om1_1); SB;
        const float om2_5 = oc2[3] * rl2; SB;
        const float om2_2 = oc2[0] * rl2; SB;
        const float om1_2 = oc1[0] * rl1; SB;
        const unsigned ow1_1 = pk2(om1_2, om1_3); SB;
        const float om2_0 = od2[0] * rl2; SB;
        const unsigned ow2_0 = pk2(om2_0, om2_1); SB;
        const float om1_4 = oc1[2] * rl1; SB;
        const unsigned ow1_2 = pk2(om1_4, om1_5); SB;
        const float om0_1 = od0[1] * rl0; SB;
        const unsigned ow0_0 = pk2(om0_0, om0_1); SB;
        const float om0_4 = oc0[2] * rl0; SB;
        const float om2_4 = oc2[2] * rl2; SB;
        const unsigned ow2_2 = pk2(om2_4, om2_5); SB;
        const unsigned ow0_2 = pk2(om0_4, om0_5); SB;
        const float om2_3 = oc2[1] * rl2; SB;
        const unsigned ow2_1 = pk2(om2_2, om2_3); SB;
        chk = max(max(chk, fbits(od0[2])), fbits(rl0)); SB;
        chk = max(max(chk, fbits(od1[2])), fbits(rl1)); SB;
        chk = max(max(chk, fbits(od2[2])), fbits(rl2)); SB;
        const f32x4 oc3 = mfma16(VA1, lp1, oa3); SB;
        *(unsigned*)(smem + ow + OOFF + 6144 + 2048) = ow2_0; SB;
        const f32x4 od3 = mfma16(VB1, lp1, ob3); SB;
        *(unsigned*)(smem + ow + OOFF + 0 + 2048) = ow0_0; SB;
        *(unsigned*)(smem + ow + OOFF + 6144) = ow2_1; *(unsigned*)(smem + ow + OOFF + 6144 + 1024) = ow2_2; SB;
        *(unsigned*)(smem + ow + OOFF + 3072) = ow1_1; *(unsigned*)(smem + ow + OOFF + 3072 + 1024) = ow1_2; SB;
        *(unsigned*)(smem + ow + OOFF + 0) = ow0_1; *(unsigned*)(smem + ow + OOFF + 0 + 1024) = ow0_2; SB;
        *(unsigned*)(smem + ow + OOFF + 3072 + 2048) = ow1_0; SB;
        const float rl3 = __builtin_amdgcn_rcpf(od3[2]); SB;
        const float om3_4 = oc3[2] * rl3; SB;
        const float om3_1 = od3[1] * rl3; SB;
        const float om3_5 = oc3[3] * rl3; SB;
        const unsigned ow3_2 = pk2(om3_4, om3_5); SB;
        const float om3_0 = od3[0] * rl3; SB;
        const unsigned ow3_0 = pk2(om3_0, om3_1); SB;
        const float om3_3 = oc3[1] * rl3; SB;
        const float om3_2 = oc3[0] * rl3; SB;
        const unsigned ow3_1 = pk2(om3_2, om3_3); SB;
        chk = max(max(chk, fbits(od3[2])), fbits(rl3)); SB;
        *(unsigned*)(smem + owl + 0 + 2048) = ow3_0; SB;
        *(unsigned*)(smem + owl + 0) = ow3_1; *(unsigned*)(smem + owl + 0 + 1024) = ow3_2; SB;
      } else if constexpr (TAIL) {
        const f32x4 ld1 = mfma16(Qf3, Kf1, ZERO4); SB;
        const f32x4 ld2 = mfma16(Qf3, Kf2, ZERO4); SB;
        const f32x4 ld3 = mfma16(Qf3, Kf3, cLD); SB;
        const f32x4 ld0 = mfma16(Qf3, Kf0, ZERO4); SB;
        const f32x4 sc2_0 = mfma16(Kf0, Qf2, ZERO4); SB;
        const f32x4 sc0_1 = mfma16(Kf1, Qf0, ZERO4); SB;
        const f32x4 sc1_2 = mfma16(Kf2, Qf1, ZERO4); SB;
        const f32x4 sc2_3 = mfma16(Kf3, Qf2, cS3); SB;
        const f32x4 sc1_1 = mfma16(Kf1, Qf1, ZERO4); SB;
        const f32x4 sc2_1 = mfma16(Kf1, Qf2, ZERO4); SB;
        const float lv0 = g0 ? ld0[3] : ld3[3]; SB;
        const f32x4 sc0_3 = mfma16(Kf3, Qf0, cS3); SB;
        const float lv1 = g1 ? ld1[3] : lv0; SB;
        const float lv2 = g2 ? ld2[3] : lv1; SB;
        const f32x4 sc2_2 = mfma16(Kf2, Qf2, ZERO4); SB;
        const float lve = __builtin_amdgcn_exp2f(lv2); SB;
        const f32x4 sc0_2 = mfma16(Kf2, Qf0, ZERO4); SB;
        const unsigned lvp = pk2(lve, lve); SB;
        const float e2_0_0 = __builtin_amdgcn_exp2f(sc2_0[0]); SB;
        const f32x4 sc1_3 = mfma16(Kf3, Qf1, cS3); SB;
        *(unsigned short*)(smem + psw) = (unsigned short)lvp; SB;
        const f32x4 sc0_0 = mfma16(Kf0, Qf0, ZERO4); SB;
        const u32x4 lp0 = lds128(smem, psr); SB;
        const u32x4 lp1 = lds128(smem, psr + 64); SB;
        const f32x4 sc1_0 = mfma16(Kf0, Qf1, ZERO4); SB;
        const float e1_1_2 = __builtin_amdgcn_exp2f(sc1_1[2]); SB;
        const float e0_1_2 = __builtin_amdgcn_exp2f(sc0_1[2]); SB;
        const float e2_3 = __builtin_amdgcn_exp2f(sc2_3[3]); SB;
        const float e2_1_2 = __builtin_amdgcn_exp2f(sc2_1[2]); SB;
        const float e2_1_3 = __builtin_amdgcn_exp2f(sc2_1[3]); SB;
        const float e0_0_0 = __builtin_amdgcn_exp2f(sc0_0[0]); SB;
        const float e0_0_1 = __builtin_amdgcn_exp2f(sc0_0[1]); SB;
        const float e0_2_2 = __builtin_amdgcn_exp2f(sc0_2[2]); SB;
        const float e0_3 = __builtin_amdgcn_exp2f(sc0_3[3]); SB;
        const float e0_2_3 = __builtin_amdgcn_exp2f(sc0_2[3]); SB;
        const float e1_2_0 = __builtin_amdgcn_exp2f(sc1_2[0]); SB;
        const float e2_0_2 = __builtin_amdgcn_exp2f(sc2_0[2]); SB;
        const float e1_3 = __builtin_amdgcn_exp2f(sc1_3[3]); SB;
        const float e1_1_3 = __builtin_amdgcn_exp2f(sc1_1[3]); SB;
        const float e2_1_1 = __builtin_amdgcn_exp2f(sc2_1[1]); SB;
        const float e2_2_0 = __builtin_amdgcn_exp2f(sc2_2[0]); SB;
        const float e2_0_1 = __builtin_amdgcn_exp2f(sc2_0[1]); SB;
        const float e1_0_2 = __builtin_amdgcn_exp2f(sc1_0[2]); SB;
        const float e0_0_3 = __builtin_amdgcn_exp2f(sc0_0[3]); SB;
        const float e0_2_0 = __builtin_amdgcn_exp2f(sc0_2[0]); SB;
        const float e1_2_3 = __builtin_amdgcn_exp2f(sc1_2[3]); SB;
        const float e1_0_1 = __builtin_amdgcn_exp2f(sc1_0[1]); SB;
        const float e0_1_3 = __builtin_amdgcn_exp2f(sc0_1[3]); SB;
        const unsigned pp0_0_0 = pk2(e0_0_0, e0_0_1); SB;
        const unsigned pp2_1_1 = pk2(e2_1_2, e2_1_3); SB;
        const float e1_1_0 = __builtin_amdgcn_exp2f(sc1_1[0]); SB;
        const unsigned pp1_3 = pk2(zf, e1_3); SB;
        const float e2_2_1 = __builtin_amdgcn_exp2f(sc2_2[1]); SB;
        const float e0_0_2 = __builtin_amdgcn_exp2f(sc0_0[2]); SB;
        const float e2_2_3 = __builtin_amdgcn_exp2f(sc2_2[3]); SB;
        const unsigned pp0_0_1 = pk2(e0_0_2, e0_0_3); SB;
        const float e1_0_3 = __builtin_amdgcn_exp2f(sc1_0[3]); SB;
        const float e1_0_0 = __builtin_amdgcn_exp2f(sc1_0[0]); SB;
        const unsigned pp2_3 = pk2(zf, e2_3); SB;
        const float e2_1_0 = __builtin_amdgcn_exp2f(sc2_1[0]); SB;
        const float e2_2_2 = __builtin_amdgcn_exp2f(sc2_2[2]); SB;
        const unsigned pp2_1_0 = pk2(e2_1_0, e2_1_1); SB;
        const unsigned pp2_0_0 = pk2(e2_0_0, e2_0_1); SB;
        const unsigned pp1_0_0 = pk2(e1_0_0, e1_0_1); SB;
        const unsigned pp1_0_1 = pk2(e1_0_2, e1_0_3); SB;
        const float e1_2_2 = __builtin_amdgcn_exp2f(sc1_2[2]); SB;
        const float e0_1_0 = __builtin_amdgcn_exp2f(sc0_1[0]); SB;
        const unsigned pp1_2_1 = pk2(e1_2_2, e1_2_3); SB;
        const unsigned pp1_1_1 = pk2(e1_1_2, e1_1_3); SB;
        const float e2_0_3 = __builtin_amdgcn_exp2f(sc2_0[3]); SB;
        const unsigned pp0_3 = pk2(zf, e0_3); SB;
        const unsigned pp2_0_1 = pk2(e2_0_2, e2_0_3); SB;
        const u32x4 PA2 = u32x4{pp2_0_0, pp2_0_1, pp2_1_0, pp2_1_1};
        const float e0_1_1 = __builtin_amdgcn_exp2f(sc0_1[1]); SB;
        const float e1_1_1 = __builtin_amdgcn_exp2f(sc1_1[1]); SB;
        const f32x4 ob2 = mfma16(VB0, PA2, ZERO4); SB;
        const unsigned pp0_1_0 = pk2(e0_1_0, e0_1_1); SB;
        const unsigned pp1_1_0 = pk2(e1_1_0, e1_1_1); SB;
        const u32x4 PA1 = u32x4{pp1_0_0, pp1_0_1, pp1_1_0, pp1_1_1};
        const f32x4 oa2 = mfma16(VA0, PA2, ZERO4); SB;
        const float e0_2_1 = __builtin_amdgcn_exp2f(sc0_2[1]); SB;
        const f32x4 oa1 = mfma16(VA0, PA1, ZERO4); SB;
        const float e1_2_1 = __builtin_amdgcn_exp2f(sc1_2[1]); SB;
        const f32x4 ob1 = mfma16(VB0, PA1, ZERO4); SB;
        const unsigned pp1_2_0 = pk2(e1_2_0, e1_2_1); SB;
        PB1[0] = pp1_2_0; PB1[1] = pp1_2_1; PB1[3] = pp1_3;
        const unsigned pp0_1_1 = pk2(e0_1_2, e0_1_3); SB;
        const u32x4 PA0 = u32x4{pp0_0_0, pp0_0_1, pp0_1_0, pp0_1_1};
        const unsigned pp2_2_1 = pk2(e2_2_2, e2_2_3); SB;
        const f32x4 od1 = mfma16(VB1, PB1, ob1); SB;
        const unsigned pp0_2_0 = pk2(e0_2_0, e0_2_1); SB;
        const unsigned pp2_2_0 = pk2(e2_2_0, e2_2_1); SB;
        PB2[0] = pp2_2_0; PB2[1] = pp2_2_1; PB2[3] = pp2_3;
        const f32x4 ob0 = mfma16(VB0, PA0, ZERO4); SB;
        const unsigned pp0_2_1 = pk2(e0_2_2, e0_2_3); SB;
        PB0[0] = pp0_2_0; PB0[1] = pp0_2_1; PB0[3] = pp0_3;
        const f32x4 od2 = mfma16(VB1, PB2, ob2); SB;
        const f32x4 ob3 = mfma16(VB0, lp0, ZERO4); SB;
        const float rl1 = __builtin_amdgcn_rcpf(od1[2]); SB;
        const f32x4 od0 = mfma16(VB1, PB0, ob0); SB;
        const float om1_1 = od1[1] * rl1; SB;
        const float om1_0 = od1[0] * rl1; SB;
        const f32x4 oa3 = mfma16(VA0, lp0, ZERO4); SB;
        const unsigned ow1_0 = pk2(om1_0, om1_1); SB;
        const float rl2 = __builtin_amdgcn_rcpf(od2[2]); SB;
        const f32x4 oc1 = mfma16(VA1, PB1, oa1); SB;
        const float om2_0 = od2[0] * rl2; SB;
        const float om2_1 = od2[1] * rl2; SB;
        const f32x4 oa0 = mfma16(VA0, PA0, ZERO4); SB;
        const float rl0 = __builtin_amdgcn_rcpf(od0[2]); SB;
        const f32x4 oc0 = mfma16(VA1, PB0, oa0); SB;
        const float om0_1 = od0[1] * rl0; SB;
        const float om0_0 = od0[0] * rl0; SB;
        const f32x4 oc2 = mfma16(VA1, PB2, oa2); SB;
        const float om1_4 = oc1[2] * rl1; SB;
        const float om1_5 = oc1[3] * rl1; SB;
        const unsigned ow0_0 = pk2(om0_0, om0_1); SB;
        const float om1_3 = oc1[1] * rl1; SB;
        const unsigned ow2_0 = pk2(om2_0, om2_1); SB;
        const float om0_3 = oc0[1] * rl0; SB;
        const float om0_2 = oc0[0] * rl0; SB;
        const unsigned ow0_1 = pk2(om0_2, om0_3); SB;
        const float om2_4 = oc2[2] * rl2; SB;
        const float om0_4 = oc0[2] * rl0; SB;
        const float om1_2 = oc1[0] * rl1; SB;
        const unsigned ow1_1 = pk2(om1_2, om1_3); SB;
        const unsigned ow1_2 = pk2(om1_4, om1_5); SB;
        const float om2_5 = oc2[3] * rl2; SB;
        const unsigned ow2_2 = pk2(om2_4, om2_5); SB;
        const float om2_3 = oc2[1] * rl2; SB;
        const float om2_2 = oc2[0] * rl2; SB;
        const unsigned ow2_1 = pk2(om2_2, om2_3); SB;
        const float om0_5 = oc0[3] * rl0; SB;
        const unsigned ow0_2 = pk2(om0_4, om0_5); SB;
        chk = max(max(chk, fbits(od0[2])), fbits(rl0)); SB;
        chk = max(max(chk, fbits(od1[2])), fbits(rl1)); SB;
        chk = max(max(chk, fbits(od2[2])), fbits(rl2)); SB;
        const f32x4 od3 = mfma16(VB1, lp1, ob3); SB;
        *(unsigned*)(smem + ow + OOFF + 3072 + 2048) = ow1_0; SB;
        const f32x4 oc3 = mfma16(VA1, lp1, oa3); SB;
        *(unsigned*)(smem + ow + OOFF + 0 + 2048) = ow0_0; SB;
        *(unsigned*)(smem + ow + OOFF + 6144 + 2048) = ow2_0; SB;
        *(unsigned*)(smem + ow + OOFF + 0) = ow0_1; *(unsigned*)(smem + ow + OOFF + 0 + 1024) = ow0_2; SB;
        *(unsigned*)(smem + ow + OOFF + 3072) = ow1_1; *(unsigned*)(smem + ow + OOFF + 3072 + 1024) = ow1_2; SB;
        *(unsigned*)(smem + ow + OOFF + 6144) = ow2_1; *(unsigned*)(smem + ow + OOFF + 6144 + 1024) = ow2_2; SB;
        const float rl3 = __builtin_amdgcn_rcpf(od3[2]); SB;
        const float om3_3 = oc3[1] * rl3; SB;
        const float om3_2 = oc3[0] * rl3; SB;
        const unsigned ow3_1 = pk2(om3_2, om3_3); SB;
        const float om3_0 = od3[0] * rl3; SB;
        const float om3_1 = od3[1] * rl3; SB;
        const unsigned ow3_0 = pk2(om3_0, om3_1); SB;
        const float om3_4 = oc3[2] * rl3; SB;
        const float om3_5 = oc3[3] * rl3; SB;
        const unsigned ow3_2 = pk2(om3_4, om3_5); SB;
        chk = max(max(chk, fbits(od3[2])), fbits(rl3)); SB;
        *(unsigned*)(smem + owl + 48 + 2048) = ow3_0; SB;
        *(unsigned*)(smem + owl + 48) = ow3_1; *(unsigned*)(smem + owl + 48 + 1024) = ow3_2; SB;
      } else if constexpr (V == 0) {
        const f32x4 ld2 = mfma16(Qf3, Kf2, ZERO4); SB;
        __builtin_amdgcn_s_setprio(3); SB;
        const u32x4 hf0_0 = lds128(smem, hb + HOFF + 0); SB;
        const u32x4 hf0_1 = lds128(smem, hb + HOFF + 1024); SB;
        __builtin_amdgcn_s_setprio(1); SB;
        const f32x4 ld0 = mfma16(Qf3, Kf0, ZERO4); SB;
        __builtin_amdgcn_s_setprio(3); SB;
        const u32x4 hf0_2 = lds128(smem, hb + HOFF + 2048); SB;
        const u32x4 hf1_0 = lds128(smem, hb + HOFF + 3072); SB;
        __builtin_amdgcn_s_setprio(1); SB;
        const f32x4 ld3 = mfma16(Qf3, Kf3, cLD); SB;
        __builtin_amdgcn_s_setprio(3); SB;
        const u32x4 hf1_1 = lds128(smem, hb + HOFF + 4096); SB;
        const u32x4 hf1_2 = lds128(smem, hb + HOFF + 5120); SB;
        __builtin_amdgcn_s_setprio(1); SB;
        const f32x4 ld1 = mfma16(Qf3, Kf1, ZERO4); SB;
        __builtin_amdgcn_s_setprio(3); SB;
        const u32x4 hf2_0 = lds128(smem, hb + HOFF + 6144); SB;
        __builtin_amdgcn_s_setprio(1); SB;
        const f32x4 sc1_0 = mfma16(Kf0, Qf1, ZERO4); SB;
        PB1[3] = ow & zu; SB;
        __builtin_amdgcn_s_setprio(3); SB;
        const u32x4 hf2_1 = lds128(smem, hb + HOFF + 7168); SB;
        __builtin_amdgcn_s_setprio(1); SB;
        const f32x4 sc0_0 = mfma16(Kf0, Qf0, ZERO4); SB;
        PB2[3] = psw & zu; SB;
        const f32x4 sc2_1 = mfma16(Kf1, Qf2, ZERO4); SB;
        __builtin_amdgcn_s_setprio(3); SB;
        const u32x4 hf2_2 = lds128(smem, hb + HOFF + 8192); SB;
        __builtin_amdgcn_s_setprio(1); SB;
        const float lv0 = g0 ? ld0[0] : ld3[0]; SB;
        const f32x4 sc1_1 = mfma16(Kf1, Qf1, ZERO4); SB;
        const float lv1 = g1 ? ld1[0] : lv0; SB;
        const float lv2 = g2 ? ld2[0] : lv1; SB;
        const float lve = __builtin_amdgcn_exp2f(lv2); SB;
        const f32x4 sc0_1 = mfma16(Kf1, Qf0, ZERO4); SB;
        const unsigned lvp = pk2(lve, lve); SB;
        const float e1_0_2 = __builtin_amdgcn_exp2f(sc1_0[2]); SB;
        const f32x4 sc0_2 = mfma16(Kf2, Qf0, ZERO4); SB;
        *(unsigned short*)(smem + psw) = (unsigned short)lvp; SB;
        const f32x4 sc2_0 = mfma16(Kf0, Qf2, ZERO4); SB;
        __builtin_amdgcn_s_setprio(3); SB;
        const u32x4 lp0 = lds128(smem, psr); SB;
        __builtin_amdgcn_s_setprio(1); SB;
        const float e2_1_1 = __builtin_amdgcn_exp2f(sc2_1[1]); SB;
        __builtin_amdgcn_s_setprio(3); SB;
        const u32x4 lp1 = lds128(smem, psr + 64); SB;
        __builtin_amdgcn_s_setprio(1); SB;
        const f32x4 sc2_3 = mfma16(Kf3, Qf2, cS3); SB;
        const float e0_1_0 = __builtin_amdgcn_exp2f(sc0_1[0]); SB;
        const f32x4 sc1_2 = mfma16(Kf2, Qf1, ZERO4); SB;
        const float e0_0_2 = __builtin_amdgcn_exp2f(sc0_0[2]); SB;
        const float e0_0_3 = __builtin_amdgcn_exp2f(sc0_0[3]); SB;
        const float e0_0_1 = __builtin_amdgcn_exp2f(sc0_0[1]); SB;
        const f32x4 sc2_2 = mfma16(Kf2, Qf2, ZERO4); SB;
        const float e1_1_2 = __builtin_amdgcn_exp2f(sc1_1[2]); SB;
        const float e2_0_3 = __builtin_amdgcn_exp2f(sc2_0[3]); SB;
        const f32x4 sc1_3 = mfma16(Kf3, Qf1, cS3); SB;
        const float e0_1_2 = __builtin_amdgcn_exp2f(sc0_1[2]); SB;
        const float e1_0_3 = __builtin_amdgcn_exp2f(sc1_0[3]); SB;
        const unsigned pp1_0_1 = pk2(e1_0_2, e1_0_3); SB;
        const f32x4 sc0_3 = mfma16(Kf3, Qf0, cS3); SB;
        const float e0_0_0 = __builtin_amdgcn_exp2f(sc0_0[0]); SB;
        const unsigned pp0_0_0 = pk2(e0_0_0, e0_0_1); SB;
        const float e2_0_1 = __builtin_amdgcn_exp2f(sc2_0[1]); SB;
        const float e1_1_3 = __builtin_amdgcn_exp2f(sc1_1[3]); SB;
        const float e2_2_0 = __builtin_amdgcn_exp2f(sc2_2[0]); SB;
        const unsigned pp1_1_1 = pk2(e1_1_2, e1_1_3); SB;
        const float e1_1_0 = __builtin_amdgcn_exp2f(sc1_1[0]); SB;
        const float e0_1_1 = __builtin_amdgcn_exp2f(sc0_1[1]); SB;
        const float e2_1_3 = __builtin_amdgcn_exp2f(sc2_1[3]); SB;
        const float e0_3 = __builtin_amdgcn_exp2f(sc0_3[0]); SB;
        const float e2_2_2 = __builtin_amdgcn_exp2f(sc2_2[2]); SB;
        const float e2_0_0 = __builtin_amdgcn_exp2f(sc2_0[0]); SB;
        const float e1_3 = __builtin_amdgcn_exp2f(sc1_3[0]); SB;
        const float e2_1_2 = __builtin_amdgcn_exp2f(sc2_1[2]); SB;
        const float e0_2_0 = __builtin_amdgcn_exp2f(sc0_2[0]); SB;
        const float e2_3 = __builtin_amdgcn_exp2f(sc2_3[0]); SB;
        const float e1_0_1 = __builtin_amdgcn_exp2f(sc1_0[1]); SB;
        const unsigned pp2_3 = pk2(e2_3, zf); SB;
        const float e1_0_0 = __builtin_amdgcn_exp2f(sc1_0[0]); SB;
        const float e1_1_1 = __builtin_amdgcn_exp2f(sc1_1[1]); SB;
        const unsigned pp0_1_0 = pk2(e0_1_0, e0_1_1); SB;
        const unsigned pp1_0_0 = pk2(e1_0_0, e1_0_1); SB;
        const unsigned pp1_1_0 = pk2(e1_1_0, e1_1_1); SB;
        const u32x4 PA1 = u32x4{pp1_0_0, pp1_0_1, pp1_1_0, pp1_1_1};
        const float e1_2_0 = __builtin_amdgcn_exp2f(sc1_2[0]); SB;
        const float e0_1_3 = __builtin_amdgcn_exp2f(sc0_1[3]); SB;
        const f32x4 oa1 = mfma16(VA0, PA1, ZERO4); SB;
        const unsigned pp0_1_1 = pk2(e0_1_2, e0_1_3); SB;
        const float e2_0_2 = __builtin_amdgcn_exp2f(sc2_0[2]); SB;
        const float e2_2_3 = __builtin_amdgcn_exp2f(sc2_2[3]); SB;
        const unsigned pp2_0_1 = pk2(e2_0_2, e2_0_3); SB;
        const f32x4 ob1 = mfma16(VB0, PA1, ZERO4); SB;
        const float e0_2_3 = __builtin_amdgcn_exp2f(sc0_2[3]); SB;
        const float e2_1_0 = __builtin_amdgcn_exp2f(sc2_1[0]); SB;
        const unsigned pp2_2_1 = pk2(e2_2_2, e2_2_3); SB;
        const float e0_2_2 = __builtin_amdgcn_exp2f(sc0_2[2]); SB;
        const unsigned pp1_3 = pk2(e1_3, zf); SB;
        const unsigned pp0_2_1 = pk2(e0_2_2, e0_2_3); SB;
        const float e2_2_1 = __builtin_amdgcn_exp2f(sc2_2[1]); SB;
        const float e1_2_1 = __builtin_amdgcn_exp2f(sc1_2[1]); SB;
        const unsigned pp1_2_0 = pk2(e1_2_0, e1_2_1); SB;
        const unsigned pp2_1_1 = pk2(e2_1_2, e2_1_3); SB;
        const float e1_2_2 = __builtin_amdgcn_exp2f(sc1_2[2]); SB;
        const unsigned pp0_0_1 = pk2(e0_0_2, e0_0_3); SB;
        const u32x4 PA0 = u32x4{pp0_0_0, pp0_0_1, pp0_1_0, pp0_1_1};
        const unsigned pp2_2_0 = pk2(e2_2_0, e2_2_1); SB;
        PB2[0] = pp2_2_0; PB2[1] = pp2_2_1; PB2[2] = pp2_3;
        const unsigned pp2_0_0 = pk2(e2_0_0, e2_0_1); SB;
        const unsigned pp2_1_0 = pk2(e2_1_0, e2_1_1); SB;
        const u32x4 PA2 = u32x4{pp2_0_0, pp2_0_1, pp2_1_0, pp2_1_1};
        const unsigned pp0_3 = pk2(e0_3, zf); SB;
        const f32x4 ob0 = mfma16(VB0, PA0, ZERO4); SB;
        const float e1_2_3 = __builtin_amdgcn_exp2f(sc1_2[3]); SB;
        const f32x4 ob2 = mfma16(VB0, PA2, ZERO4); SB;
        const unsigned pp1_2_1 = pk2(e1_2_2, e1_2_3); SB;
        PB1[0] = pp1_2_0; PB1[1] = pp1_2_1; PB1[2] = pp1_3;
        const f32x4 od2 = mfma16(VB1, PB2, ob2); SB;
        const f32x4 oa2 = mfma16(VA0, PA2, ZERO4); SB;
        const f32x4 oa3 = mfma16(VA0, lp0, ZERO4); SB;
        const float e0_2_1 = __builtin_amdgcn_exp2f(sc0_2[1]); SB;
        const f32x4 od1 = mfma16(VB1, PB1, ob1); SB;
        const float rl2 = __builtin_amdgcn_rcpf(od2[2]); SB;
        const unsigned pp0_2_0 = pk2(e0_2_0, e0_2_1); SB;
        PB0[3] = hb & zu; SB;
        PB0[0] = pp0_2_0; PB0[1] = pp0_2_1; PB0[2] = pp0_3;
        const f32x4 ob3 = mfma16(VB0, lp0, ZERO4); SB;
        const float rl1 = __builtin_amdgcn_rcpf(od1[2]); SB;
        const f32x4 oc2 = mfma16(VA1, PB2, oa2); SB;
        const float om1_0 = od1[0] * rl1; SB;
        const float om1_1 = od1[1] * rl1; SB;
        const f32x4 oa0 = mfma16(VA0, PA0, ZERO4); SB;
        const unsigned ow1_0 = pk2(om1_0, om1_1); SB;
        const float om2_0 = od2[0] * rl2; SB;
        const f32x4 oc0 = mfma16(VA1, PB0, oa0); SB;
        *(unsigned*)(smem + ow + OOFF + 3072 + 2048) = ow1_0; SB;
        const f32x4 od0 = mfma16(VB1, PB0, ob0); SB;
        const float om2_3 = oc2[1] * rl2; SB;
        const float om2_4 = oc2[2] * rl2; SB;
        const f32x4 oc1 = mfma16(VA1, PB1, oa1); SB;
        const float om2_5 = oc2[3] * rl2; SB;
        const float om2_2 = oc2[0] * rl2; SB;
        const f32x4 xx0_0 = mfma16(WX[0], hf0_0, ZERO4); SB;
        const unsigned ow2_1 = pk2(om2_2, om2_3); SB;
        const float om2_1 = od2[1] * rl2; SB;
        const f32x4 xx1_0 = mfma16(WX[0], hf1_0, ZERO4); SB;
        const float rl0 = __builtin_amdgcn_rcpf(od0[2]); SB;
        const f32x4 q0_0 = mfma16(WQ[0], hf0_0, ZERO4); SB;
        const float om0_2 = oc0[0] * rl0; SB;
        const float om1_2 = oc1[0] * rl1; SB;
        const f32x4 v10_0 = mfma16(hf0_0, WV1[0], cV1); SB;
        const unsigned ow2_0 = pk2(om2_0, om2_1); SB;
        const f32x4 xx1_1 = mfma16(WX[1], hf1_1, xx1_0); SB;
        const float om0_4 = oc0[2] * rl0; SB;
        const float om0_5 = oc0[3] * rl0; SB;
        const float om0_3 = oc0[1] * rl0; SB;
        const unsigned ow0_1 = pk2(om0_2, om0_3); SB;
        const f32x4 xx0_1 = mfma16(WX[1], hf0_1, xx0_0); SB;
        const float om1_4 = oc1[2] * rl1; SB;
        const f32x4 q2_0 = mfma16(WQ[0], hf2_0, ZERO4); SB;
        *(unsigned*)(smem + ow + OOFF + 6144 + 2048) = ow2_0; SB;
        const f32x4 q0_1 = mfma16(WQ[1], hf0_1, q0_0); SB;
        const unsigned ow0_2 = pk2(om0_4, om0_5); SB;
        const float om0_0 = od0[0] * rl0; SB;
        const f32x4 v10_1 = mfma16(hf0_1, WV1[1], v10_0); SB;
        *(unsigned*)(smem + ow + OOFF + 0) = ow0_1; *(unsigned*)(smem + ow + OOFF + 0 + 1024) = ow0_2; SB;
        const f32x4 kk0_0 = mfma16(WK[0], hf0_0, ZERO4); SB;
        const float om1_5 = oc1[3] * rl1; SB;
        const unsigned ow1_2 = pk2(om1_4, om1_5); SB;
        const f32x4 kk0_1 = mfma16(WK[1], hf0_1, kk0_0); SB;
        const float om1_3 = oc1[1] * rl1; SB;
        const unsigned ow1_1 = pk2(om1_2, om1_3); SB;
        const f32x4 xx2_0 = mfma16(WX[0], hf2_0, ZERO4); SB;
        *(unsigned*)(smem + ow + OOFF + 3072) = ow1_1; *(unsigned*)(smem + ow + OOFF + 3072 + 1024) = ow1_2; SB;
        const f32x4 xx2_1 = mfma16(WX[1], hf2_1, xx2_0); SB;
        const float om0_1 = od0[1] * rl0; SB;
        const unsigned ow0_0 = pk2(om0_0, om0_1); SB;
        const f32x4 xx2_2 = mfma16(WX[2], hf2_2, xx2_1); SB;
        const unsigned ow2_2 = pk2(om2_4, om2_5); SB;
        chk = max(max(chk, fbits(od0[2])), fbits(rl0)); SB;
        const f32x4 v02_0 = mfma16(hf2_0, WV0[0], ZERO4); SB;
        *(unsigned*)(smem + ow + OOFF + 6144) = ow2_1; *(unsigned*)(smem + ow + OOFF + 6144 + 1024) = ow2_2; SB;
        const f32x4 kk2_0 = mfma16(WK[0], hf2_0, ZERO4); SB;
        *(unsigned*)(smem + ow + OOFF + 0 + 2048) = ow0_0; SB;
        const f32x4 q2_1 = mfma16(WQ[1], hf2_1, q2_0); SB;
        chk = max(max(chk, fbits(od1[2])), fbits(rl1)); SB;
        const f32x4 v12_0 = mfma16(hf2_0, WV1[0], cV1); SB;
        const unsigned xa2 = pk2(xx2_2[0], xx2_2[1]); SB;
        const unsigned xc2 = pk2(xx2_2[2], xx2_2[3]); SB;
        const f32x4 od3 = mfma16(VB1, lp1, ob3); SB;
        const auto sw2 = __builtin_amdgcn_permlane32_swap(xa2, xc2, false, false); SB;
        const f32x4 q1_0 = mfma16(WQ[0], hf1_0, ZERO4); SB;
        const f32x4 oc3 = mfma16(VA1, lp1, oa3); SB;
        const f32x4 kk1_0 = mfma16(WK[0], hf1_0, ZERO4); SB;
        const f32x4 kk1_1 = mfma16(WK[1], hf1_1, kk1_0); SB;
        const f32x4 v01_0 = mfma16(hf1_0, WV0[0], ZERO4); SB;
        const f32x4 v12_1 = mfma16(hf2_1, WV1[1], v12_0); SB;
        chk = max(max(chk, fbits(od2[2])), fbits(rl2)); SB;
        const f32x4 v00_0 = mfma16(hf0_0, WV0[0], ZERO4); SB;
        const float rl3 = __builtin_amdgcn_rcpf(od3[2]); SB;
        const f32x4 v11_0 = mfma16(hf1_0, WV1[0], cV1); SB;
        const f32x4 v02_1 = mfma16(hf2_1, WV0[1], v02_0); SB;
        const float om3_5 = oc3[3] * rl3; SB;
        const float om3_0 = od3[0] * rl3; SB;
        const f32x4 v01_1 = mfma16(hf1_1, WV0[1], v01_0); SB;
        const float om3_2 = oc3[0] * rl3; SB;
        const float om3_4 = oc3[2] * rl3; SB;
        const f32x4 q1_1 = mfma16(WQ[1], hf1_1, q1_0); SB;
        const unsigned ow3_2 = pk2(om3_4, om3_5); SB;
        const float om3_1 = od3[1] * rl3; SB;
        const f32x4 xx0_2 = mfma16(WX[2], hf0_2, xx0_1); SB;
        const unsigned ow3_0 = pk2(om3_0, om3_1); SB;
        const float om3_3 = oc3[1] * rl3; SB;
        const f32x4 v00_1 = mfma16(hf0_1, WV0[1], v00_0); SB;
        const unsigned ow3_1 = pk2(om3_2, om3_3); SB;
        chk = max(max(chk, fbits(od3[2])), fbits(rl3)); SB;
        const f32x4 kk2_1 = mfma16(WK[1], hf2_1, kk2_0); SB;
        *(unsigned*)(smem + owl + 0 + 2048) = ow3_0; SB;
        const f32x4 v11_1 = mfma16(hf1_1, WV1[1], v11_0); SB;
        *(unsigned*)(smem + owl + 0) = ow3_1; *(unsigned*)(smem + owl + 0 + 1024) = ow3_2; SB;
        const f32x4 v12_2 = mfma16(hf2_2, WV1[2], v12_1); SB;
        const unsigned xc0 = pk2(xx0_2[2], xx0_2[3]); SB;
        const unsigned xa0 = pk2(xx0_2[0], xx0_2[1]); SB;
        const f32x4 v00_2 = mfma16(hf0_2, WV0[2], v00_1); SB;
        const auto sw0 = __builtin_amdgcn_permlane32_swap(xa0, xc0, false, false); SB;
        const f32x4 q2_2 = mfma16(WQ[2], hf2_2, q2_1); SB;
        VB1[1] = pk2(v12_2[2], v12_2[3]); SB;
        const f32x4 v10_2 = mfma16(hf0_2, WV1[2], v10_1); SB;
        VB1[0] = pk2(v12_2[0], v12_2[1]); SB;
        VA0[1] = pk2(v00_2[2], v00_2[3]); SB;
        const f32x4 xx1_2 = mfma16(WX[2], hf1_2, xx1_1); SB;
        VA0[0] = pk2(v00_2[0], v00_2[1]); SB;
        const unsigned qf2_0 = pk2(q2_2[0], q2_2[1]); SB;
        const f32x4 kk2_2 = mfma16(WK[2], hf2_2, kk2_1); SB;
        const unsigned qf2_1 = pk2(q2_2[2], q2_2[3]); SB;
        Qf2[0] = qf2_0; Qf2[1] = qf2_1; Qf2[2] = sw2[0];
        const f32x4 q0_2 = mfma16(WQ[2], hf0_2, q0_1); SB;
        VB0[1] = pk2(v10_2[2], v10_2[3]); SB;
        VB0[0] = pk2(v10_2[0], v10_2[1]); SB;
        const f32x4 v11_2 = mfma16(hf1_2, WV1[2], v11_1); SB;
        const unsigned xc1 = pk2(xx1_2[2], xx1_2[3]); SB;
        const unsigned xa1 = pk2(xx1_2[0], xx1_2[1]); SB;
        const f32x4 kk0_2 = mfma16(WK[2], hf0_2, kk0_1); SB;
        const auto sw1 = __builtin_amdgcn_permlane32_swap(xa1, xc1, false, false); SB;
        const f32x4 kk1_2 = mfma16(WK[2], hf1_2, kk1_1); SB;
        const unsigned qf0_0 = pk2(q0_2[0], q0_2[1]); SB;
        const unsigned qf0_1 = pk2(q0_2[2], q0_2[3]); SB;
        Qf0[0] = qf0_0; Qf0[1] = qf0_1; Qf0[2] = sw0[0];
        const f32x4 v01_2 = mfma16(hf1_2, WV0[2], v01_1); SB;
        const unsigned kf2_0 = pk2(kk2_2[0], kk2_2[1]); SB;
        VB0[2] = pk2(v11_2[0], v11_2[1]); SB;
        const f32x4 v02_2 = mfma16(hf2_2, WV0[2], v02_1); SB;
        const unsigned kf0_1 = pk2(kk0_2[2], kk0_2[3]); SB;
        const unsigned kf0_0 = pk2(kk0_2[0], kk0_2[1]); SB;
        Kf0[0] = kf0_0; Kf0[1] = kf0_1; Kf0[2] = sw0[1];
        const f32x4 q1_2 = mfma16(WQ[2], hf1_2, q1_1); SB;
        const unsigned kf1_0 = pk2(kk1_2[0], kk1_2[1]); SB;
        const unsigned kf1_1 = pk2(kk1_2[2], kk1_2[3]); SB;
        Kf1[0] = kf1_0; Kf1[1] = kf1_1; Kf1[2] = sw1[1];
        VB0[3] = pk2(v11_2[2], v11_2[3]); SB;
        const unsigned kf2_1 = pk2(kk2_2[2], kk2_2[3]); SB;
        Kf2[0] = kf2_0; Kf2[1] = kf2_1; Kf2[2] = sw2[1];
        VA0[3] = pk2(v01_2[2], v01_2[3]); SB;
        VA0[2] = pk2(v01_2[0], v01_2[1]); SB;
        VA1[1] = pk2(v02_2[2], v02_2[3]); SB;
        VA1[0] = pk2(v02_2[0], v02_2[1]); SB;
        const unsigned qf1_0 = pk2(q1_2[0], q1_2[1]); SB;
        const unsigned qf1_1 = pk2(q1_2[2], q1_2[3]); SB;
        Qf1[0] = qf1_0; Qf1[1] = qf1_1; Qf1[2] = sw1[0];
      } else if constexpr (V == 1) {
        const f32x4 sc1_1 = mfma16(Kf1, Qf1, ZERO4); SB;
        __builtin_amdgcn_s_setprio(3); SB;
        const u32x4 hf0_0 = lds128(smem, hb + HOFF + 0); SB;
        const u32x4 hf0_1 = lds128(smem, hb + HOFF + 1024); SB;
        __builtin_amdgcn_s_setprio(1); SB;
        const f32x4 ld0 = mfma16(Qf3, Kf0, ZERO4); SB;
        __builtin_amdgcn_s_setprio(3); SB;
        const u32x4 hf0_2 = lds128(smem, hb + HOFF + 2048); SB;
        const u32x4 hf1_0 = lds128(smem, hb + HOFF + 3072); SB;
        __builtin_amdgcn_s_setprio(1); SB;
        const f32x4 ld3 = mfma16(Qf3, Kf3, cLD); SB;
        __builtin_amdgcn_s_setprio(3); SB;
        const u32x4 hf1_1 = lds128(smem, hb + HOFF + 4096); SB;
        const u32x4 hf1_2 = lds128(smem, hb + HOFF + 5120); SB;
        __builtin_amdgcn_s_setprio(1); SB;
        const f32x4 ld2 = mfma16(Qf3, Kf2, ZERO4); SB;
        __builtin_amdgcn_s_setprio(3); SB;
        const u32x4 hf2_0 = lds128(smem, hb + HOFF + 6144); SB;
        const u32x4 hf2_1 = lds128(smem, hb + HOFF + 7168); SB;
        __builtin_amdgcn_s_setprio(1); SB;
        const f32x4 sc0_1 = mfma16(Kf1, Qf0, ZERO4); SB;
        const f32x4 ld1 = mfma16(Qf3, Kf1, ZERO4); SB;
        __builtin_amdgcn_s_setprio(3); SB;
        const u32x4 hf2_2 = lds128(smem, hb + HOFF + 8192); SB;
        __builtin_amdgcn_s_setprio(1); SB;
        const float lv0 = g0 ? ld0[1] : ld3[1]; SB;
        const f32x4 sc1_0 = mfma16(Kf0, Qf1, ZERO4); SB;
        const float lv1 = g1 ? ld1[1] : lv0; SB;
        const float lv2 = g2 ? ld2[1] : lv1; SB;
        const f32x4 sc2_0 = mfma16(Kf0, Qf2, ZERO4); SB;
        const f32x4 sc0_0 = mfma16(Kf0, Qf0, ZERO4); SB;
        const float e1_1_3 = __builtin_amdgcn_exp2f(sc1_1[3]); SB;
        const float e0_1_0 = __builtin_amdgcn_exp2f(sc0_1[0]); SB;
        const f32x4 sc0_2 = mfma16(Kf2, Qf0, ZERO4); SB;
        const float lve = __builtin_amdgcn_exp2f(lv2); SB;
        const f32x4 sc0_3 = mfma16(Kf3, Qf0, cS3); SB;
        const float e1_1_2 = __builtin_amdgcn_exp2f(sc1_1[2]); SB;
        const unsigned lvp = pk2(lve, lve); SB;
        const f32x4 sc1_2 = mfma16(Kf2, Qf1, ZERO4); SB;
        *(unsigned short*)(smem + psw) = (unsigned short)lvp; SB;
        const f32x4 sc2_1 = mfma16(Kf1, Qf2, ZERO4); SB;
        __builtin_amdgcn_s_setprio(3); SB;
        const u32x4 lp0 = lds128(smem, psr); SB;
        const u32x4 lp1 = lds128(smem, psr + 64); SB;
        __builtin_amdgcn_s_setprio(1); SB;
        const f32x4 sc2_3 = mfma16(Kf3, Qf2, cS3); SB;
        const float e2_0_1 = __builtin_amdgcn_exp2f(sc2_0[1]); SB;
        const float e1_0_0 = __builtin_amdgcn_exp2f(sc1_0[0]); SB;
        const f32x4 sc2_2 = mfma16(Kf2, Qf2, ZERO4); SB;
        const float e2_1_1 = __builtin_amdgcn_exp2f(sc2_1[1]); SB;
        const f32x4 sc1_3 = mfma16(Kf3, Qf1, cS3); SB;
        const float e2_1_3 = __builtin_amdgcn_exp2f(sc2_1[3]); SB;
        const unsigned pp1_1_1 = pk2(e1_1_2, e1_1_3); SB;
        const float e1_1_0 = __builtin_amdgcn_exp2f(sc1_1[0]); SB;
        const float e0_2_0 = __builtin_amdgcn_exp2f(sc0_2[0]); SB;
        const float e0_2_1 = __builtin_amdgcn_exp2f(sc0_2[1]); SB;
        const float e0_2_2 = __builtin_amdgcn_exp2f(sc0_2[2]); SB;
        const float e2_2_1 = __builtin_amdgcn_exp2f(sc2_2[1]); SB;
        const float e0_1_2 = __builtin_amdgcn_exp2f(sc0_1[2]); SB;
        const float e0_2_3 = __builtin_amdgcn_exp2f(sc0_2[3]); SB;
        const float e1_0_1 = __builtin_amdgcn_exp2f(sc1_0[1]); SB;
        const float e1_0_3 = __builtin_amdgcn_exp2f(sc1_0[3]); SB;
        const float e0_3 = __builtin_amdgcn_exp2f(sc0_3[1]); SB;
        const float e2_0_3 = __builtin_amdgcn_exp2f(sc2_0[3]); SB;
        const unsigned pp0_3 = pk2(zf, e0_3); SB;
        const float e0_0_1 = __builtin_amdgcn_exp2f(sc0_0[1]); SB;
        const float e2_2_2 = __builtin_amdgcn_exp2f(sc2_2[2]); SB;
        const float e2_1_0 = __builtin_amdgcn_exp2f(sc2_1[0]); SB;
        const float e1_2_1 = __builtin_amdgcn_exp2f(sc1_2[1]); SB;
        const unsigned pp2_1_0 = pk2(e2_1_0, e2_1_1); SB;
        const float e2_0_0 = __builtin_amdgcn_exp2f(sc2_0[0]); SB;
        const float e2_1_2 = __builtin_amdgcn_exp2f(sc2_1[2]); SB;
        const float e1_1_1 = __builtin_amdgcn_exp2f(sc1_1[1]); SB;
        const float e0_1_3 = __builtin_amdgcn_exp2f(sc0_1[3]); SB;
        const unsigned pp1_1_0 = pk2(e1_1_0, e1_1_1); SB;
        const float e0_1_1 = __builtin_amdgcn_exp2f(sc0_1[1]); SB;
        const float e1_0_2 = __builtin_amdgcn_exp2f(sc1_0[2]); SB;
        const float e1_2_0 = __builtin_amdgcn_exp2f(sc1_2[0]); SB;
        const unsigned pp1_0_1 = pk2(e1_0_2, e1_0_3); SB;
        const float e0_0_2 = __builtin_amdgcn_exp2f(sc0_0[2]); SB;
        const float e0_0_0 = __builtin_amdgcn_exp2f(sc0_0[0]); SB;
        const float e2_0_2 = __builtin_amdgcn_exp2f(sc2_0[2]); SB;
        const float e1_2_2 = __builtin_amdgcn_exp2f(sc1_2[2]); SB;
        const unsigned pp2_0_1 = pk2(e2_0_2, e2_0_3); SB;
        const unsigned pp2_0_0 = pk2(e2_0_0, e2_0_1); SB;
        const unsigned pp0_0_0 = pk2(e0_0_0, e0_0_1); SB;
        const float e0_0_3 = __builtin_amdgcn_exp2f(sc0_0[3]); SB;
        const unsigned pp0_1_0 = pk2(e0_1_0, e0_1_1); SB;
        const unsigned pp0_0_1 = pk2(e0_0_2, e0_0_3); SB;
        const float e2_2_3 = __builtin_amdgcn_exp2f(sc2_2[3]); SB;
        const float e2_2_0 = __builtin_amdgcn_exp2f(sc2_2[0]); SB;
        const unsigned pp0_1_1 = pk2(e0_1_2, e0_1_3); SB;
        const u32x4 PA0 = u32x4{pp0_0_0, pp0_0_1, pp0_1_0, pp0_1_1};
        const unsigned pp2_1_1 = pk2(e2_1_2, e2_1_3); SB;
        const u32x4 PA2 = u32x4{pp2_0_0, pp2_0_1, pp2_1_0, pp2_1_1};
        const unsigned pp2_2_0 = pk2(e2_2_0, e2_2_1); SB;
        const f32x4 ob0 = mfma16(VB0, PA0, ZERO4); SB;
        const unsigned pp1_0_0 = pk2(e1_0_0, e1_0_1); SB;
        const u32x4 PA1 = u32x4{pp1_0_0, pp1_0_1, pp1_1_0, pp1_1_1};
        const unsigned pp0_2_1 = pk2(e0_2_2, e0_2_3); SB;
        const f32x4 oa2 = mfma16(VA0, PA2, ZERO4); SB;
        const unsigned pp1_2_0 = pk2(e1_2_0, e1_2_1); SB;
        const float e1_2_3 = __builtin_amdgcn_exp2f(sc1_2[3]); SB;
        const f32x4 oa1 = mfma16(VA0, PA1, ZERO4); SB;
        const unsigned pp1_2_1 = pk2(e1_2_2, e1_2_3); SB;
        const unsigned pp0_2_0 = pk2(e0_2_0, e0_2_1); SB;
        PB0[0] = pp0_2_0; PB0[1] = pp0_2_1; PB0[2] = pp0_3;
        const f32x4 ob2 = mfma16(VB0, PA2, ZERO4); SB;
        const unsigned pp2_2_1 = pk2(e2_2_2, e2_2_3); SB;
        const float e1_3 = __builtin_amdgcn_exp2f(sc1_3[1]); SB;
        const f32x4 ob1 = mfma16(VB0, PA1, ZERO4); SB;
        const float e2_3 = __builtin_amdgcn_exp2f(sc2_3[1]); SB;
        const f32x4 od0 = mfma16(VB1, PB0, ob0); SB;
        const unsigned pp2_3 = pk2(zf, e2_3); SB;
        PB2[0] = pp2_2_0; PB2[1] = pp2_2_1; PB2[2] = pp2_3;
        const f32x4 ob3 = mfma16(VB0, lp0, ZERO4); SB;
        const f32x4 oa3 = mfma16(VA0, lp0, ZERO4); SB;
        const unsigned pp1_3 = pk2(zf, e1_3); SB;
        PB1[0] = pp1_2_0; PB1[1] = pp1_2_1; PB1[2] = pp1_3;
        const f32x4 od2 = mfma16(VB1, PB2, ob2); SB;
        const f32x4 od3 = mfma16(VB1, lp1, ob3); SB;
        const float rl0 = __builtin_amdgcn_rcpf(od0[2]); SB;
        const f32x4 oc2 = mfma16(VA1, PB2, oa2); SB;
        const float om0_1 = od0[1] * rl0; SB;
        const float om0_0 = od0[0] * rl0; SB;
        const f32x4 oc3 = mfma16(VA1, lp1, oa3); SB;
        const float rl2 = __builtin_amdgcn_rcpf(od2[2]); SB;
        const f32x4 oa0 = mfma16(VA0, PA0, ZERO4); SB;
        const float rl3 = __builtin_amdgcn_rcpf(od3[2]); SB;
        const f32x4 oc0 = mfma16(VA1, PB0, oa0); SB;
        const float om3_1 = od3[1] * rl3; SB;
        const float om2_4 = oc2[2] * rl2; SB;
        const f32x4 od1 = mfma16(VB1, PB1, ob1); SB;
        const float om2_0 = od2[0] * rl2; SB;
        const f32x4 q2_0 = mfma16(WQ[0], hf2_0, ZERO4); SB;
        const f32x4 oc1 = mfma16(VA1, PB1, oa1); SB;
        const float om3_2 = oc3[0] * rl3; SB;
        const float om3_3 = oc3[1] * rl3; SB;
        const unsigned ow3_1 = pk2(om3_2, om3_3); SB;
        const f32x4 xx2_0 = mfma16(WX[0], hf2_0, ZERO4); SB;
        const float rl1 = __builtin_amdgcn_rcpf(od1[2]); SB;
        const float om3_5 = oc3[3] * rl3; SB;
        const f32x4 xx1_0 = mfma16(WX[0], hf1_0, ZERO4); SB;
        const float om0_3 = oc0[1] * rl0; SB;
        const f32x4 xx2_1 = mfma16(WX[1], hf2_1, xx2_0); SB;
        const float om1_1 = od1[1] * rl1; SB;
        const float om1_4 = oc1[2] * rl1; SB;
        const float om1_3 = oc1[1] * rl1; SB;
        const float om1_5 = oc1[3] * rl1; SB;
        const f32x4 xx1_1 = mfma16(WX[1], hf1_1, xx1_0); SB;
        const float om0_4 = oc0[2] * rl0; SB;
        const float om3_4 = oc3[2] * rl3; SB;
        const f32x4 q2_1 = mfma16(WQ[1], hf2_1, q2_0); SB;
        const unsigned ow3_2 = pk2(om3_4, om3_5); SB;
        const float om0_5 = oc0[3] * rl0; SB;
        const f32x4 xx2_2 = mfma16(WX[2], hf2_2, xx2_1); SB;
        const unsigned ow0_2 = pk2(om0_4, om0_5); SB;
        const float om1_0 = od1[0] * rl1; SB;
        *(unsigned*)(smem + owl + 16) = ow3_1; *(unsigned*)(smem + owl + 16 + 1024) = ow3_2; SB;
        const f32x4 q0_0 = mfma16(WQ[0], hf0_0, ZERO4); SB;
        const float om2_3 = oc2[1] * rl2; SB;
        const float om3_0 = od3[0] * rl3; SB;
        const f32x4 q2_2 = mfma16(WQ[2], hf2_2, q2_1); SB;
        const unsigned ow1_0 = pk2(om1_0, om1_1); SB;
        const float om2_1 = od2[1] * rl2; SB;
        const f32x4 xx1_2 = mfma16(WX[2], hf1_2, xx1_1); SB;
        const f32x4 q0_1 = mfma16(WQ[1], hf0_1, q0_0); SB;
        const unsigned ow3_0 = pk2(om3_0, om3_1); SB;
        const float om2_2 = oc2[0] * rl2; SB;
        const f32x4 q0_2 = mfma16(WQ[2], hf0_2, q0_1); SB;
        const unsigned ow2_1 = pk2(om2_2, om2_3); SB;
        const float om0_2 = oc0[0] * rl0; SB;
        const unsigned ow0_1 = pk2(om0_2, om0_3); SB;
        const f32x4 xx0_0 = mfma16(WX[0], hf0_0, ZERO4); SB;
        *(unsigned*)(smem + owl + 16 + 2048) = ow3_0; SB;
        const f32x4 xx0_1 = mfma16(WX[1], hf0_1, xx0_0); SB;
        const unsigned ow1_2 = pk2(om1_4, om1_5); SB;
        const float om2_5 = oc2[3] * rl2; SB;
        const f32x4 q1_0 = mfma16(WQ[0], hf1_0, ZERO4); SB;
        const unsigned ow2_2 = pk2(om2_4, om2_5); SB;
        const float om1_2 = oc1[0] * rl1; SB;
        const f32x4 v12_0 = mfma16(hf2_0, WV1[0], cV1); SB;
        *(unsigned*)(smem + ow + OOFF + 6144) = ow2_1; *(unsigned*)(smem + ow + OOFF + 6144 + 1024) = ow2_2; SB;
        const f32x4 q1_1 = mfma16(WQ[1], hf1_1, q1_0); SB;
        const unsigned ow1_1 = pk2(om1_2, om1_3); SB;
        const unsigned ow0_0 = pk2(om0_0, om0_1); SB;
        const f32x4 v00_0 = mfma16(hf0_0, WV0[0], ZERO4); SB;
        *(unsigned*)(smem + ow + OOFF + 3072) = ow1_1; *(unsigned*)(smem + ow + OOFF + 3072 + 1024) = ow1_2; SB;
        const f32x4 v12_1 = mfma16(hf2_1, WV1[1], v12_0); SB;
        const unsigned ow2_0 = pk2(om2_0, om2_1); SB;
        const f32x4 kk0_0 = mfma16(WK[0], hf0_0, ZERO4); SB;
        *(unsigned*)(smem + ow + OOFF + 3072 + 2048) = ow1_0; SB;
        const unsigned xa1 = pk2(xx1_2[0], xx1_2[1]); SB;
        const f32x4 v02_0 = mfma16(hf2_0, WV0[0], ZERO4); SB;
        *(unsigned*)(smem + ow + OOFF + 6144 + 2048) = ow2_0; SB;
        const f32x4 xx0_2 = mfma16(WX[2], hf0_2, xx0_1); SB;
        *(unsigned*)(smem + ow + OOFF + 0 + 2048) = ow0_0; SB;
        const f32x4 v02_1 = mfma16(hf2_1, WV0[1], v02_0); SB;
        *(unsigned*)(smem + ow + OOFF + 0) = ow0_1; *(unsigned*)(smem + ow + OOFF + 0 + 1024) = ow0_2; SB;
        const f32x4 kk0_1 = mfma16(WK[1], hf0_1, kk0_0); SB;
        const unsigned xc2 = pk2(xx2_2[2], xx2_2[3]); SB;
        const unsigned xa2 = pk2(xx2_2[0], xx2_2[1]); SB;
        const unsigned xc1 = pk2(xx1_2[2], xx1_2[3]); SB;
        const f32x4 v11_0 = mfma16(hf1_0, WV1[0], cV1); SB;
        const unsigned xa0 = pk2(xx0_2[0], xx0_2[1]); SB;
        const unsigned xc0 = pk2(xx0_2[2], xx0_2[3]); SB;
        const f32x4 kk2_0 = mfma16(WK[0], hf2_0, ZERO4); SB;
        const auto sw0 = __builtin_amdgcn_permlane32_swap(xa0, xc0, false, false); SB;
        const f32x4 kk2_1 = mfma16(WK[1], hf2_1, kk2_0); SB;
        const auto sw2 = __builtin_amdgcn_permlane32_swap(xa2, xc2, false, false); SB;
        const f32x4 kk1_0 = mfma16(WK[0], hf1_0, ZERO4); SB;
        const auto sw1 = __builtin_amdgcn_permlane32_swap(xa1, xc1, false, false); SB;
        const f32x4 v10_0 = mfma16(hf0_0, WV1[0], cV1); SB;
        const unsigned qf0_1 = pk2(q0_2[2], q0_2[3]); SB;
        const unsigned qf0_0 = pk2(q0_2[0], q0_2[1]); SB;
        Qf0[0] = qf0_0; Qf0[1] = qf0_1; Qf0[2] = sw0[0];
        const f32x4 v10_1 = mfma16(hf0_1, WV1[1], v10_0); SB;
        const unsigned qf2_0 = pk2(q2_2[0], q2_2[1]); SB;
        const unsigned qf2_1 = pk2(q2_2[2], q2_2[3]); SB;
        Qf2[0] = qf2_0; Qf2[1] = qf2_1; Qf2[2] = sw2[0];
        const f32x4 v01_0 = mfma16(hf1_0, WV0[0], ZERO4); SB;
        chk = max(max(chk, fbits(od0[2])), fbits(rl0)); SB;
        const f32x4 v11_1 = mfma16(hf1_1, WV1[1], v11_0); SB;
        const f32x4 v00_1 = mfma16(hf0_1, WV0[1], v00_0); SB;
        const f32x4 v00_2 = mfma16(hf0_2, WV0[2], v00_1); SB;
        const f32x4 kk1_1 = mfma16(WK[1], hf1_1, kk1_0); SB;
        const f32x4 v12_2 = mfma16(hf2_2, WV1[2], v12_1); SB;
        chk = max(max(chk, fbits(od1[2])), fbits(rl1)); SB;
        const f32x4 v01_1 = mfma16(hf1_1, WV0[1], v01_0); SB;
        const f32x4 kk0_2 = mfma16(WK[2], hf0_2, kk0_1); SB;
        const f32x4 q1_2 = mfma16(WQ[2], hf1_2, q1_1); SB;
        const f32x4 v02_2 = mfma16(hf2_2, WV0[2], v02_1); SB;
        chk = max(max(chk, fbits(od2[2])), fbits(rl2)); SB;
        chk = max(max(chk, fbits(od3[2])), fbits(rl3)); SB;
        const f32x4 kk1_2 = mfma16(WK[2], hf1_2, kk1_1); SB;
        VA0[0] = pk2(v00_2[0], v00_2[1]); SB;
        const f32x4 v11_2 = mfma16(hf1_2, WV1[2], v11_1); SB;
        VB1[1] = pk2(v12_2[2], v12_2[3]); SB;
        VB1[0] = pk2(v12_2[0], v12_2[1]); SB;
        const f32x4 kk2_2 = mfma16(WK[2], hf2_2, kk2_1); SB;
        const f32x4 v01_2 = mfma16(hf1_2, WV0[2], v01_1); SB;
        const unsigned qf1_1 = pk2(q1_2[2], q1_2[3]); SB;
        const f32x4 v10_2 = mfma16(hf0_2, WV1[2], v10_1); SB;
        const unsigned kf0_0 = pk2(kk0_2[0], kk0_2[1]); SB;
        const unsigned kf1_1 = pk2(kk1_2[2], kk1_2[3]); SB;
        const unsigned qf1_0 = pk2(q1_2[0], q1_2[1]); SB;
        Qf1[0] = qf1_0; Qf1[1] = qf1_1; Qf1[2] = sw1[0];
        VB0[2] = pk2(v11_2[0], v11_2[1]); SB;
        VA0[2] = pk2(v01_2[0], v01_2[1]); SB;
        const unsigned kf2_0 = pk2(kk2_2[0], kk2_2[1]); SB;
        VA0[1] = pk2(v00_2[2], v00_2[3]); SB;
        VB0[0] = pk2(v10_2[0], v10_2[1]); SB;
        VA0[3] = pk2(v01_2[2], v01_2[3]); SB;
        VA1[1] = pk2(v02_2[2], v02_2[3]); SB;
        VB0[3] = pk2(v11_2[2], v11_2[3]); SB;
        VA1[0] = pk2(v02_2[0], v02_2[1]); SB;
        const unsigned kf0_1 = pk2(kk0_2[2], kk0_2[3]); SB;
        Kf0[0] = kf0_0; Kf0[1] = kf0_1; Kf0[2] = sw0[1];
        const unsigned kf1_0 = pk2(kk1_2[0], kk1_2[1]); SB;
        Kf1[0] = kf1_0; Kf1[1] = kf1_1; Kf1[2] = sw1[1];
        const unsigned kf2_1 = pk2(kk2_2[2], kk2_2[3]); SB;
        Kf2[0] = kf2_0; Kf2[1] = kf2_1; Kf2[2] = sw2[1];
        VB0[1] = pk2(v10_2[2], v10_2[3]); SB;
      } else if constexpr (V == 2) {
        const f32x4 ld2 = mfma16(Qf3, Kf2, ZERO4); SB;
        __builtin_amdgcn_s_setprio(3); SB;
        const u32x4 hf0_0 = lds128(smem, hb + HOFF + 0); SB;
        const u32x4 hf0_1 = lds128(smem, hb + HOFF + 1024); SB;
        __builtin_amdgcn_s_setprio(1); SB;
        const f32x4 ld0 = mfma16(Qf3, Kf0, ZERO4); SB;
        __builtin_amdgcn_s_setprio(3); SB;
        const u32x4 hf0_2 = lds128(smem, hb + HOFF + 2048); SB;
        const u32x4 hf1_0 = lds128(smem, hb + HOFF + 3072); SB;
        __builtin_amdgcn_s_setprio(1); SB;
        const f32x4 ld3 = mfma16(Qf3, Kf3, cLD); SB;
        __builtin_amdgcn_s_setprio(3); SB;
        const u32x4 hf1_1 = lds128(smem, hb + HOFF + 4096); SB;
        const u32x4 hf1_2 = lds128(smem, hb + HOFF + 5120); SB;
        __builtin_amdgcn_s_setprio(1); SB;
        const f32x4 ld1 = mfma16(Qf3, Kf1, ZERO4); SB;
        __builtin_amdgcn_s_setprio(3); SB;
        const u32x4 hf2_0 = lds128(smem, hb + HOFF + 6144); SB;
        __builtin_amdgcn_s_setprio(1); SB;
        const f32x4 sc1_0 = mfma16(Kf0, Qf1, ZERO4); SB;
        __builtin_amdgcn_s_setprio(3); SB;
        const u32x4 hf2_1 = lds128(smem, hb + HOFF + 7168); SB;
        __builtin_amdgcn_s_setprio(1); SB;
        PB1[2] = ow & zu; SB;
        PB2[2] = psw & zu; SB;
        const f32x4 sc2_1 = mfma16(Kf1, Qf2, ZERO4); SB;
        __builtin_amdgcn_s_setprio(3); SB;
        const u32x4 hf2_2 = lds128(smem, hb + HOFF + 8192); SB;
        __builtin_amdgcn_s_setprio(1); SB;
        const float lv0 = g0 ? ld0[2] : ld3[2]; SB;
        const f32x4 sc0_0 = mfma16(Kf0, Qf0, ZERO4); SB;
        const float lv1 = g1 ? ld1[2] : lv0; SB;
        const float lv2 = g2 ? ld2[2] : lv1; SB;
        const f32x4 sc1_1 = mfma16(Kf1, Qf1, ZERO4); SB;
        const float lve = __builtin_amdgcn_exp2f(lv2); SB;
        const f32x4 sc0_1 = mfma16(Kf1, Qf0, ZERO4); SB;
        const unsigned lvp = pk2(lve, lve); SB;
        const float e1_0_2 = __builtin_amdgcn_exp2f(sc1_0[2]); SB;
        const f32x4 sc0_2 = mfma16(Kf2, Qf0, ZERO4); SB;
        *(unsigned short*)(smem + psw) = (unsigned short)lvp; SB;
        const f32x4 sc2_0 = mfma16(Kf0, Qf2, ZERO4); SB;
        __builtin_amdgcn_s_setprio(3); SB;
        const u32x4 lp0 = lds128(smem, psr); SB;
        const u32x4 lp1 = lds128(smem, psr + 64); SB;
        __builtin_amdgcn_s_setprio(1); SB;
        const f32x4 sc2_3 = mfma16(Kf3, Qf2, cS3); SB;
        const float e0_1_0 = __builtin_amdgcn_exp2f(sc0_1[0]); SB;
        const f32x4 sc1_2 = mfma16(Kf2, Qf1, ZERO4); SB;
        const float e0_0_2 = __builtin_amdgcn_exp2f(sc0_0[2]); SB;
        const f32x4 sc2_2 = mfma16(Kf2, Qf2, ZERO4); SB;
        const float e1_1_2 = __builtin_amdgcn_exp2f(sc1_1[2]); SB;
        const f32x4 sc0_3 = mfma16(Kf3, Qf0, cS3); SB;
        const float e2_0_3 = __builtin_amdgcn_exp2f(sc2_0[3]); SB;
        const f32x4 sc1_3 = mfma16(Kf3, Qf1, cS3); SB;
        const float e0_1_2 = __builtin_amdgcn_exp2f(sc0_1[2]); SB;
        const float e0_0_0 = __builtin_amdgcn_exp2f(sc0_0[0]); SB;
        const float e0_0_3 = __builtin_amdgcn_exp2f(sc0_0[3]); SB;
        const float e1_0_3 = __builtin_amdgcn_exp2f(sc1_0[3]); SB;
        const float e0_0_1 = __builtin_amdgcn_exp2f(sc0_0[1]); SB;
        const unsigned pp1_0_1 = pk2(e1_0_2, e1_0_3); SB;
        const float e2_1_1 = __builtin_amdgcn_exp2f(sc2_1[1]); SB;
        const unsigned pp0_0_0 = pk2(e0_0_0, e0_0_1); SB;
        const float e2_0_1 = __builtin_amdgcn_exp2f(sc2_0[1]); SB;
        const float e1_1_3 = __builtin_amdgcn_exp2f(sc1_1[3]); SB;
        const float e2_2_0 = __builtin_amdgcn_exp2f(sc2_2[0]); SB;
        const float e2_3 = __builtin_amdgcn_exp2f(sc2_3[2]); SB;
        const unsigned pp1_1_1 = pk2(e1_1_2, e1_1_3); SB;
        const float e0_1_1 = __builtin_amdgcn_exp2f(sc0_1[1]); SB;
        const float e2_1_3 = __builtin_amdgcn_exp2f(sc2_1[3]); SB;
        const float e0_3 = __builtin_amdgcn_exp2f(sc0_3[2]); SB;
        const float e2_2_2 = __builtin_amdgcn_exp2f(sc2_2[2]); SB;
        const float e2_1_2 = __builtin_amdgcn_exp2f(sc2_1[2]); SB;
        const unsigned pp0_1_0 = pk2(e0_1_0, e0_1_1); SB;
        const float e0_2_0 = __builtin_amdgcn_exp2f(sc0_2[0]); SB;
        const float e2_0_0 = __builtin_amdgcn_exp2f(sc2_0[0]); SB;
        const float e1_3 = __builtin_amdgcn_exp2f(sc1_3[2]); SB;
        const float e1_0_1 = __builtin_amdgcn_exp2f(sc1_0[1]); SB;
        const unsigned pp2_3 = pk2(e2_3, zf); SB;
        const float e1_0_0 = __builtin_amdgcn_exp2f(sc1_0[0]); SB;
        const float e1_1_1 = __builtin_amdgcn_exp2f(sc1_1[1]); SB;
        const unsigned pp1_0_0 = pk2(e1_0_0, e1_0_1); SB;
        const float e1_1_0 = __builtin_amdgcn_exp2f(sc1_1[0]); SB;
        const float e2_2_3 = __builtin_amdgcn_exp2f(sc2_2[3]); SB;
        const unsigned pp1_1_0 = pk2(e1_1_0, e1_1_1); SB;
        const u32x4 PA1 = u32x4{pp1_0_0, pp1_0_1, pp1_1_0, pp1_1_1};
        const float e1_2_0 = __builtin_amdgcn_exp2f(sc1_2[0]); SB;
        const float e0_1_3 = __builtin_amdgcn_exp2f(sc0_1[3]); SB;
        const f32x4 oa1 = mfma16(VA0, PA1, ZERO4); SB;
        const unsigned pp0_1_1 = pk2(e0_1_2, e0_1_3); SB;
        const float e2_0_2 = __builtin_amdgcn_exp2f(sc2_0[2]); SB;
        const f32x4 ob1 = mfma16(VB0, PA1, ZERO4); SB;
        const unsigned pp2_0_1 = pk2(e2_0_2, e2_0_3); SB;
        const float e0_2_3 = __builtin_amdgcn_exp2f(sc0_2[3]); SB;
        const unsigned pp2_2_1 = pk2(e2_2_2, e2_2_3); SB;
        const float e2_1_0 = __builtin_amdgcn_exp2f(sc2_1[0]); SB;
        const float e0_2_2 = __builtin_amdgcn_exp2f(sc0_2[2]); SB;
        const unsigned pp1_3 = pk2(e1_3, zf); SB;
        const unsigned pp2_1_1 = pk2(e2_1_2, e2_1_3); SB;
        const unsigned pp0_0_1 = pk2(e0_0_2, e0_0_3); SB;
        const u32x4 PA0 = u32x4{pp0_0_0, pp0_0_1, pp0_1_0, pp0_1_1};
        const unsigned pp2_0_0 = pk2(e2_0_0, e2_0_1); SB;
        const unsigned pp2_1_0 = pk2(e2_1_0, e2_1_1); SB;
        const u32x4 PA2 = u32x4{pp2_0_0, pp2_0_1, pp2_1_0, pp2_1_1};
        const unsigned pp0_2_1 = pk2(e0_2_2, e0_2_3); SB;
        const float e2_2_1 = __builtin_amdgcn_exp2f(sc2_2[1]); SB;
        const float e1_2_1 = __builtin_amdgcn_exp2f(sc1_2[1]); SB;
        const unsigned pp2_2_0 = pk2(e2_2_0, e2_2_1); SB;
        PB2[0] = pp2_2_0; PB2[1] = pp2_2_1; PB2[3] = pp2_3;
        const unsigned pp1_2_0 = pk2(e1_2_0, e1_2_1); SB;
        const float e1_2_2 = __builtin_amdgcn_exp2f(sc1_2[2]); SB;
        const f32x4 ob0 = mfma16(VB0, PA0, ZERO4); SB;
        const float e1_2_3 = __builtin_amdgcn_exp2f(sc1_2[3]); SB;
        const f32x4 ob2 = mfma16(VB0, PA2, ZERO4); SB;
        const unsigned pp1_2_1 = pk2(e1_2_2, e1_2_3); SB;
        PB1[0] = pp1_2_0; PB1[1] = pp1_2_1; PB1[3] = pp1_3;
        const unsigned pp0_3 = pk2(e0_3, zf); SB;
        const f32x4 od2 = mfma16(VB1, PB2, ob2); SB;
        const float e0_2_1 = __builtin_amdgcn_exp2f(sc0_2[1]); SB;
        const f32x4 od1 = mfma16(VB1, PB1, ob1); SB;
        const unsigned pp0_2_0 = pk2(e0_2_0, e0_2_1); SB;
        PB0[2] = hb & zu; SB;
        PB0[0] = pp0_2_0; PB0[1] = pp0_2_1; PB0[3] = pp0_3;
        const f32x4 oa2 = mfma16(VA0, PA2, ZERO4); SB;
        const f32x4 oa3 = mfma16(VA0, lp0, ZERO4); SB;
        const float rl2 = __builtin_amdgcn_rcpf(od2[2]); SB;
        const f32x4 ob3 = mfma16(VB0, lp0, ZERO4); SB;
        const float rl1 = __builtin_amdgcn_rcpf(od1[2]); SB;
        const f32x4 oc2 = mfma16(VA1, PB2, oa2); SB;
        const float om1_0 = od1[0] * rl1; SB;
        const float om1_1 = od1[1] * rl1; SB;
        const f32x4 oa0 = mfma16(VA0, PA0, ZERO4); SB;
        const unsigned ow1_0 = pk2(om1_0, om1_1); SB;
        const float om2_0 = od2[0] * rl2; SB;
        const f32x4 oc0 = mfma16(VA1, PB0, oa0); SB;
        *(unsigned*)(smem + ow + OOFF + 3072 + 2048) = ow1_0; SB;
        const f32x4 od0 = mfma16(VB1, PB0, ob0); SB;
        const float om2_3 = oc2[1] * rl2; SB;
        const float om2_4 = oc2[2] * rl2; SB;
        const f32x4 oc1 = mfma16(VA1, PB1, oa1); SB;
        const float om2_5 = oc2[3] * rl2; SB;
        const float om2_2 = oc2[0] * rl2; SB;
        const f32x4 xx0_0 = mfma16(WX[0], hf0_0, ZERO4); SB;
        const unsigned ow2_1 = pk2(om2_2, om2_3); SB;
        const float om2_1 = od2[1] * rl2; SB;
        const f32x4 xx1_0 = mfma16(WX[0], hf1_0, ZERO4); SB;
        const float rl0 = __builtin_amdgcn_rcpf(od0[2]); SB;
        const f32x4 xx0_1 = mfma16(WX[1], hf0_1, xx0_0); SB;
        const float om0_5 = oc0[3] * rl0; SB;
        const f32x4 xx1_1 = mfma16(WX[1], hf1_1, xx1_0); SB;
        const float om0_4 = oc0[2] * rl0; SB;
        const f32x4 q2_0 = mfma16(WQ[0], hf2_0, ZERO4); SB;
        const float om1_4 = oc1[2] * rl1; SB;
        const f32x4 kk0_0 = mfma16(WK[0], hf0_0, ZERO4); SB;
        const float om1_5 = oc1[3] * rl1; SB;
        const unsigned ow1_2 = pk2(om1_4, om1_5); SB;
        const unsigned ow2_0 = pk2(om2_0, om2_1); SB;
        *(unsigned*)(smem + ow + OOFF + 6144 + 2048) = ow2_0; SB;
        const f32x4 q0_0 = mfma16(WQ[0], hf0_0, ZERO4); SB;
        const float om0_2 = oc0[0] * rl0; SB;
        const float om1_2 = oc1[0] * rl1; SB;
        const f32x4 v10_0 = mfma16(hf0_0, WV1[0], cV1); SB;
        const float om0_3 = oc0[1] * rl0; SB;
        const unsigned ow0_1 = pk2(om0_2, om0_3); SB;
        const f32x4 q0_1 = mfma16(WQ[1], hf0_1, q0_0); SB;
        const unsigned ow0_2 = pk2(om0_4, om0_5); SB;
        const f32x4 v10_1 = mfma16(hf0_1, WV1[1], v10_0); SB;
        *(unsigned*)(smem + ow + OOFF + 0) = ow0_1; *(unsigned*)(smem + ow + OOFF + 0 + 1024) = ow0_2; SB;
        const float om0_0 = od0[0] * rl0; SB;
        const f32x4 kk0_1 = mfma16(WK[1], hf0_1, kk0_0); SB;
        const float om1_3 = oc1[1] * rl1; SB;
        const unsigned ow1_1 = pk2(om1_2, om1_3); SB;
        const f32x4 xx2_0 = mfma16(WX[0], hf2_0, ZERO4); SB;
        *(unsigned*)(smem + ow + OOFF + 3072) = ow1_1; *(unsigned*)(smem + ow + OOFF + 3072 + 1024) = ow1_2; SB;
        const f32x4 xx2_1 = mfma16(WX[1], hf2_1, xx2_0); SB;
        const float om0_1 = od0[1] * rl0; SB;
        const unsigned ow0_0 = pk2(om0_0, om0_1); SB;
        const f32x4 xx2_2 = mfma16(WX[2], hf2_2, xx2_1); SB;
        const unsigned ow2_2 = pk2(om2_4, om2_5); SB;
        chk = max(max(chk, fbits(od0[2])), fbits(rl0)); SB;
        const f32x4 v02_0 = mfma16(hf2_0, WV0[0], ZERO4); SB;
        const f32x4 kk2_0 = mfma16(WK[0], hf2_0, ZERO4); SB;
        *(unsigned*)(smem + ow + OOFF + 0 + 2048) = ow0_0; SB;
        const f32x4 q2_1 = mfma16(WQ[1], hf2_1, q2_0); SB;
        chk = max(max(chk, fbits(od1[2])), fbits(rl1)); SB;
        const unsigned xa2 = pk2(xx2_2[0], xx2_2[1]); SB;
        const f32x4 v12_0 = mfma16(hf2_0, WV1[0], cV1); SB;
        *(unsigned*)(smem + ow + OOFF + 6144) = ow2_1; *(unsigned*)(smem + ow + OOFF + 6144 + 1024) = ow2_2; SB;
        const f32x4 v00_0 = mfma16(hf0_0, WV0[0], ZERO4); SB;
        chk = max(max(chk, fbits(od2[2])), fbits(rl2)); SB;
        const f32x4 v01_0 = mfma16(hf1_0, WV0[0], ZERO4); SB;
        const f32x4 od3 = mfma16(VB1, lp1, ob3); SB;
        const unsigned xc2 = pk2(xx2_2[2], xx2_2[3]); SB;
        const f32x4 oc3 = mfma16(VA1, lp1, oa3); SB;
        const f32x4 kk1_0 = mfma16(WK[0], hf1_0, ZERO4); SB;
        const f32x4 v11_0 = mfma16(hf1_0, WV1[0], cV1); SB;
        const auto sw2 = __builtin_amdgcn_permlane32_swap(xa2, xc2, false, false); SB;
        const f32x4 q1_0 = mfma16(WQ[0], hf1_0, ZERO4); SB;
        const f32x4 kk1_1 = mfma16(WK[1], hf1_1, kk1_0); SB;
        const f32x4 v12_1 = mfma16(hf2_1, WV1[1], v12_0); SB;
        const float rl3 = __builtin_amdgcn_rcpf(od3[2]); SB;
        const f32x4 v02_1 = mfma16(hf2_1, WV0[1], v02_0); SB;
        const float om3_5 = oc3[3] * rl3; SB;
        const float om3_0 = od3[0] * rl3; SB;
        const f32x4 v01_1 = mfma16(hf1_1, WV0[1], v01_0); SB;
        const float om3_2 = oc3[0] * rl3; SB;
        const float om3_4 = oc3[2] * rl3; SB;
        const f32x4 q1_1 = mfma16(WQ[1], hf1_1, q1_0); SB;
        const unsigned ow3_2 = pk2(om3_4, om3_5); SB;
        const float om3_1 = od3[1] * rl3; SB;
        const f32x4 xx0_2 = mfma16(WX[2], hf0_2, xx0_1); SB;
        const unsigned ow3_0 = pk2(om3_0, om3_1); SB;
        const float om3_3 = oc3[1] * rl3; SB;
        const f32x4 v00_1 = mfma16(hf0_1, WV0[1], v00_0); SB;
        const unsigned ow3_1 = pk2(om3_2, om3_3); SB;
        chk = max(max(chk, fbits(od3[2])), fbits(rl3)); SB;
        const f32x4 kk2_1 = mfma16(WK[1], hf2_1, kk2_0); SB;
        *(unsigned*)(smem + owl + 32 + 2048) = ow3_0; SB;
        const f32x4 v11_1 = mfma16(hf1_1, WV1[1], v11_0); SB;
        const unsigned xa0 = pk2(xx0_2[0], xx0_2[1]); SB;
        *(unsigned*)(smem + owl + 32) = ow3_1; *(unsigned*)(smem + owl + 32 + 1024) = ow3_2; SB;
        const f32x4 v00_2 = mfma16(hf0_2, WV0[2], v00_1); SB;
        const f32x4 v12_2 = mfma16(hf2_2, WV1[2], v12_1); SB;
        const unsigned xc0 = pk2(xx0_2[2], xx0_2[3]); SB;
        const auto sw0 = __builtin_amdgcn_permlane32_swap(xa0, xc0, false, false); SB;
        const f32x4 q2_2 = mfma16(WQ[2], hf2_2, q2_1); SB;
        VB1[1] = pk2(v12_2[2], v12_2[3]); SB;
        const f32x4 v10_2 = mfma16(hf0_2, WV1[2], v10_1); SB;
        VB1[0] = pk2(v12_2[0], v12_2[1]); SB;
        const f32x4 xx1_2 = mfma16(WX[2], hf1_2, xx1_1); SB;
        const unsigned qf2_0 = pk2(q2_2[0], q2_2[1]); SB;
        const f32x4 kk2_2 = mfma16(WK[2], hf2_2, kk2_1); SB;
        VA0[0] = pk2(v00_2[0], v00_2[1]); SB;
        const unsigned qf2_1 = pk2(q2_2[2], q2_2[3]); SB;
        Qf2[0] = qf2_0; Qf2[1] = qf2_1; Qf2[2] = sw2[0];
        VA0[1] = pk2(v00_2[2], v00_2[3]); SB;
        const f32x4 q0_2 = mfma16(WQ[2], hf0_2, q0_1); SB;
        const f32x4 v02_2 = mfma16(hf2_2, WV0[2], v02_1); SB;
        VB0[1] = pk2(v10_2[2], v10_2[3]); SB;
        VB0[0] = pk2(v10_2[0], v10_2[1]); SB;
        const unsigned xa1 = pk2(xx1_2[0], xx1_2[1]); SB;
        const f32x4 kk0_2 = mfma16(WK[2], hf0_2, kk0_1); SB;
        const f32x4 v11_2 = mfma16(hf1_2, WV1[2], v11_1); SB;
        const unsigned xc1 = pk2(xx1_2[2], xx1_2[3]); SB;
        const unsigned kf2_1 = pk2(kk2_2[2], kk2_2[3]); SB;
        const auto sw1 = __builtin_amdgcn_permlane32_swap(xa1, xc1, false, false); SB;
        const unsigned qf0_1 = pk2(q0_2[2], q0_2[3]); SB;
        const f32x4 v01_2 = mfma16(hf1_2, WV0[2], v01_1); SB;
        const f32x4 kk1_2 = mfma16(WK[2], hf1_2, kk1_1); SB;
        const unsigned qf0_0 = pk2(q0_2[0], q0_2[1]); SB;
        Qf0[0] = qf0_0; Qf0[1] = qf0_1; Qf0[2] = sw0[0];
        const unsigned kf2_0 = pk2(kk2_2[0], kk2_2[1]); SB;
        Kf2[0] = kf2_0; Kf2[1] = kf2_1; Kf2[2] = sw2[1];
        VB0[2] = pk2(v11_2[0], v11_2[1]); SB;
        const unsigned kf0_1 = pk2(kk0_2[2], kk0_2[3]); SB;
        const unsigned kf0_0 = pk2(kk0_2[0], kk0_2[1]); SB;
        Kf0[0] = kf0_0; Kf0[1] = kf0_1; Kf0[2] = sw0[1];
        const f32x4 q1_2 = mfma16(WQ[2], hf1_2, q1_1); SB;
        VA1[1] = pk2(v02_2[2], v02_2[3]); SB;
        VA0[2] = pk2(v01_2[0], v01_2[1]); SB;
        VB0[3] = pk2(v11_2[2], v11_2[3]); SB;
        const unsigned kf1_0 = pk2(kk1_2[0], kk1_2[1]); SB;
        const unsigned kf1_1 = pk2(kk1_2[2], kk1_2[3]); SB;
        Kf1[0] = kf1_0; Kf1[1] = kf1_1; Kf1[2] = sw1[1];
        VA1[0] = pk2(v02_2[0], v02_2[1]); SB;
        const unsigned qf1_1 = pk2(q1_2[2], q1_2[3]); SB;
        VA0[3] = pk2(v01_2[2], v01_2[3]); SB;
        const unsigned qf1_0 = pk2(q1_2[0], q1_2[1]); SB;
        Qf1[0] = qf1_0; Qf1[1] = qf1_1; Qf1[2] = sw1[0];
      } else {
        const f32x4 ld1 = mfma16(Qf3, Kf1, ZERO4); SB;
        __builtin_amdgcn_s_setprio(3); SB;
        const u32x4 hf0_0 = lds128(smem, hb + HOFF + 0); SB;
        const u32x4 hf0_1 = lds128(smem, hb + HOFF + 1024); SB;
        __builtin_amdgcn_s_setprio(1); SB;
        const f32x4 ld2 = mfma16(Qf3, Kf2, ZERO4); SB;
        __builtin_amdgcn_s_setprio(3); SB;
        const u32x4 hf0_2 = lds128(smem, hb + HOFF + 2048); SB;
        const u32x4 hf1_0 = lds128(smem, hb + HOFF + 3072); SB;
        __builtin_amdgcn_s_setprio(1); SB;
        const f32x4 ld0 = mfma16(Qf3, Kf0, ZERO4); SB;
        __builtin_amdgcn_s_setprio(3); SB;
        const u32x4 hf1_1 = lds128(smem, hb + HOFF + 4096); SB;
        const u32x4 hf1_2 = lds128(smem, hb + HOFF + 5120); SB;
        __builtin_amdgcn_s_setprio(1); SB;
        const f32x4 ld3 = mfma16(Qf3, Kf3, cLD); SB;
        __builtin_amdgcn_s_setprio(3); SB;
        const u32x4 hf2_0 = lds128(smem, hb + HOFF + 6144); SB;
        __builtin_amdgcn_s_setprio(1); SB;
        const f32x4 sc1_1 = mfma16(Kf1, Qf1, ZERO4); SB;
        __builtin_amdgcn_s_setprio(3); SB;
        const u32x4 hf2_1 = lds128(smem, hb + HOFF + 7168); SB;
        __builtin_amdgcn_s_setprio(1); SB;
        const f32x4 sc0_0 = mfma16(Kf0, Qf0, ZERO4); SB;
        __builtin_amdgcn_s_setprio(3); SB;
        const u32x4 hf2_2 = lds128(smem, hb + HOFF + 8192); SB;
        __builtin_amdgcn_s_setprio(1); SB;
        const f32x4 sc2_1 = mfma16(Kf1, Qf2, ZERO4); SB;
        __builtin_amdgcn_s_setprio(3); SB;
        const u32x4 hf3_0 = lds128(smem, hl + 0); SB;
        __builtin_amdgcn_s_setprio(1); SB;
        const f32x4 sc2_2 = mfma16(Kf2, Qf2, ZERO4); SB;
        __builtin_amdgcn_s_setprio(3); SB;
        const u32x4 hf3_1 = lds128(smem, hl + 1024); SB;
        __builtin_amdgcn_s_setprio(1); SB;
        const float lv0 = g0 ? ld0[3] : ld3[3]; SB;
        const f32x4 sc1_2 = mfma16(Kf2, Qf1, ZERO4); SB;
        __builtin_amdgcn_s_setprio(3); SB;
        const u32x4 hf3_2 = lds128(smem, hl + 2048); SB;
        __builtin_amdgcn_s_setprio(1); SB;
        const float lv1 = g1 ? ld1[3] : lv0; SB;
        const f32x4 sc1_0 = mfma16(Kf0, Qf1, ZERO4); SB;
        const float lv2 = g2 ? ld2[3] : lv1; SB;
        const float lve = __builtin_amdgcn_exp2f(lv2); SB;
        const float e0_0_0 = __builtin_amdgcn_exp2f(sc0_0[0]); SB;
        const f32x4 sc0_2 = mfma16(Kf2, Qf0, ZERO4); SB;
        const unsigned lvp = pk2(lve, lve); SB;
        const f32x4 sc2_0 = mfma16(Kf0, Qf2, ZERO4); SB;
        const float e1_2_2 = __builtin_amdgcn_exp2f(sc1_2[2]); SB;
        const float e2_2_1 = __builtin_amdgcn_exp2f(sc2_2[1]); SB;
        *(unsigned short*)(smem + psw) = (unsigned short)lvp; SB;
        const f32x4 sc1_3 = mfma16(Kf3, Qf1, cS3); SB;
        __builtin_amdgcn_s_setprio(3); SB;
        const u32x4 lp0 = lds128(smem, psr); SB;
        const u32x4 lp1 = lds128(smem, psr + 64); SB;
        __builtin_amdgcn_s_setprio(1); SB;
        const f32x4 sc2_3 = mfma16(Kf3, Qf2, cS3); SB;
        const float e1_1_2 = __builtin_amdgcn_exp2f(sc1_1[2]); SB;
        const float e0_0_3 = __builtin_amdgcn_exp2f(sc0_0[3]); SB;
        const float e2_0_2 = __builtin_amdgcn_exp2f(sc2_0[2]); SB;
        const float e1_1_3 = __builtin_amdgcn_exp2f(sc1_1[3]); SB;
        const float e2_0_3 = __builtin_amdgcn_exp2f(sc2_0[3]); SB;
        const float e2_0_0 = __builtin_amdgcn_exp2f(sc2_0[0]); SB;
        const unsigned pp1_1_1 = pk2(e1_1_2, e1_1_3); SB;
        const float e2_1_0 = __builtin_amdgcn_exp2f(sc2_1[0]); SB;
        const float e0_2_0 = __builtin_amdgcn_exp2f(sc0_2[0]); SB;
        const float e1_2_3 = __builtin_amdgcn_exp2f(sc1_2[3]); SB;
        const float e0_2_1 = __builtin_amdgcn_exp2f(sc0_2[1]); SB;
        const float e2_2_3 = __builtin_amdgcn_exp2f(sc2_2[3]); SB;
        const float e1_1_0 = __builtin_amdgcn_exp2f(sc1_1[0]); SB;
        const float e1_0_0 = __builtin_amdgcn_exp2f(sc1_0[0]); SB;
        const float e2_3 = __builtin_amdgcn_exp2f(sc2_3[3]); SB;
        const unsigned pp1_2_1 = pk2(e1_2_2, e1_2_3); SB;
        const float e2_0_1 = __builtin_amdgcn_exp2f(sc2_0[1]); SB;
        const float e1_0_3 = __builtin_amdgcn_exp2f(sc1_0[3]); SB;
        const unsigned pp2_0_0 = pk2(e2_0_0, e2_0_1); SB;
        const float e2_1_3 = __builtin_amdgcn_exp2f(sc2_1[3]); SB;
        const float e0_2_2 = __builtin_amdgcn_exp2f(sc0_2[2]); SB;
        const float e1_0_1 = __builtin_amdgcn_exp2f(sc1_0[1]); SB;
        const float e0_2_3 = __builtin_amdgcn_exp2f(sc0_2[3]); SB;
        const float e2_1_2 = __builtin_amdgcn_exp2f(sc2_1[2]); SB;
        const float e1_3 = __builtin_amdgcn_exp2f(sc1_3[3]); SB;
        const unsigned pp2_3 = pk2(zf, e2_3); SB;
        const float e1_1_1 = __builtin_amdgcn_exp2f(sc1_1[1]); SB;
        const float e1_2_0 = __builtin_amdgcn_exp2f(sc1_2[0]); SB;
        const float e2_1_1 = __builtin_amdgcn_exp2f(sc2_1[1]); SB;
        const unsigned pp2_0_1 = pk2(e2_0_2, e2_0_3); SB;
        const unsigned pp2_1_0 = pk2(e2_1_0, e2_1_1); SB;
        const f32x4 sc0_1 = mfma16(Kf1, Qf0, ZERO4); SB;
        const unsigned pp1_1_0 = pk2(e1_1_0, e1_1_1); SB;
        const unsigned pp1_3 = pk2(zf, e1_3); SB;
        const float e1_2_1 = __builtin_amdgcn_exp2f(sc1_2[1]); SB;
        const f32x4 sc0_3 = mfma16(Kf3, Qf0, cS3); SB;
        const float e0_0_2 = __builtin_amdgcn_exp2f(sc0_0[2]); SB;
        const float e2_2_2 = __builtin_amdgcn_exp2f(sc2_2[2]); SB;
        const unsigned pp0_0_1 = pk2(e0_0_2, e0_0_3); SB;
        const unsigned pp2_2_1 = pk2(e2_2_2, e2_2_3); SB;
        const float e0_1_0 = __builtin_amdgcn_exp2f(sc0_1[0]); SB;
        const float e0_1_2 = __builtin_amdgcn_exp2f(sc0_1[2]); SB;
        const float e0_1_1 = __builtin_amdgcn_exp2f(sc0_1[1]); SB;
        const unsigned pp0_1_0 = pk2(e0_1_0, e0_1_1); SB;
        const float e0_3 = __builtin_amdgcn_exp2f(sc0_3[3]); SB;
        const float e0_1_3 = __builtin_amdgcn_exp2f(sc0_1[3]); SB;
        const unsigned pp2_1_1 = pk2(e2_1_2, e2_1_3); SB;
        const u32x4 PA2 = u32x4{pp2_0_0, pp2_0_1, pp2_1_0, pp2_1_1};
        const unsigned pp0_1_1 = pk2(e0_1_2, e0_1_3); SB;
        const float e0_0_1 = __builtin_amdgcn_exp2f(sc0_0[1]); SB;
        const f32x4 oa2 = mfma16(VA0, PA2, ZERO4); SB;
        const unsigned pp0_0_0 = pk2(e0_0_0, e0_0_1); SB;
        const u32x4 PA0 = u32x4{pp0_0_0, pp0_0_1, pp0_1_0, pp0_1_1};
        const float e2_2_0 = __builtin_amdgcn_exp2f(sc2_2[0]); SB;
        const f32x4 ob2 = mfma16(VB0, PA2, ZERO4); SB;
        const unsigned pp2_2_0 = pk2(e2_2_0, e2_2_1); SB;
        PB2[0] = pp2_2_0; PB2[1] = pp2_2_1; PB2[3] = pp2_3;
        const unsigned pp1_0_0 = pk2(e1_0_0, e1_0_1); SB;
        const f32x4 oa0 = mfma16(VA0, PA0, ZERO4); SB;
        const float e1_0_2 = __builtin_amdgcn_exp2f(sc1_0[2]); SB;
        const f32x4 od2 = mfma16(VB1, PB2, ob2); SB;
        const unsigned pp1_0_1 = pk2(e1_0_2, e1_0_3); SB;
        const u32x4 PA1 = u32x4{pp1_0_0, pp1_0_1, pp1_1_0, pp1_1_1};
        const unsigned pp0_3 = pk2(zf, e0_3); SB;
        const f32x4 oc2 = mfma16(VA1, PB2, oa2); SB;
        const unsigned pp0_2_0 = pk2(e0_2_0, e0_2_1); SB;
        const unsigned pp1_2_0 = pk2(e1_2_0, e1_2_1); SB;
        PB1[0] = pp1_2_0; PB1[1] = pp1_2_1; PB1[3] = pp1_3;
        const f32x4 ob1 = mfma16(VB0, PA1, ZERO4); SB;
        const f32x4 ob0 = mfma16(VB0, PA0, ZERO4); SB;
        const unsigned pp0_2_1 = pk2(e0_2_2, e0_2_3); SB;
        PB0[0] = pp0_2_0; PB0[1] = pp0_2_1; PB0[3] = pp0_3;
        const f32x4 ob3 = mfma16(VB0, lp0, ZERO4); SB;
        const float rl2 = __builtin_amdgcn_rcpf(od2[2]); SB;
        const f32x4 oa3 = mfma16(VA0, lp0, ZERO4); SB;
        const float om2_1 = od2[1] * rl2; SB;
        const float om2_0 = od2[0] * rl2; SB;
        const float om2_2 = oc2[0] * rl2; SB;
        const float om2_5 = oc2[3] * rl2; SB;
        const f32x4 oc0 = mfma16(VA1, PB0, oa0); SB;
        const float om2_3 = oc2[1] * rl2; SB;
        const unsigned ow2_0 = pk2(om2_0, om2_1); SB;
        const f32x4 od1 = mfma16(VB1, PB1, ob1); SB;
        const float om2_4 = oc2[2] * rl2; SB;
        const unsigned ow2_2 = pk2(om2_4, om2_5); SB;
        const f32x4 oa1 = mfma16(VA0, PA1, ZERO4); SB;
        const unsigned ow2_1 = pk2(om2_2, om2_3); SB;
        const f32x4 oc3 = mfma16(VA1, lp1, oa3); SB;
        *(unsigned*)(smem + ow + OOFF + 6144) = ow2_1; *(unsigned*)(smem + ow + OOFF + 6144 + 1024) = ow2_2; SB;
        const f32x4 oc1 = mfma16(VA1, PB1, oa1); SB;
        const float rl1 = __builtin_amdgcn_rcpf(od1[2]); SB;
        const f32x4 od3 = mfma16(VB1, lp1, ob3); SB;
        const float om1_1 = od1[1] * rl1; SB;
        const float om1_0 = od1[0] * rl1; SB;
        const f32x4 od0 = mfma16(VB1, PB0, ob0); SB;
        const unsigned ow1_0 = pk2(om1_0, om1_1); SB;
        const f32x4 xx3_0 = mfma16(WX[0], hf3_0, ZERO4); SB;
        *(unsigned*)(smem + ow + OOFF + 6144 + 2048) = ow2_0; SB;
        const f32x4 v13_0 = mfma16(hf3_0, WV1[0], cV1); SB;
        const float om1_4 = oc1[2] * rl1; SB;
        const float rl3 = __builtin_amdgcn_rcpf(od3[2]); SB;
        const f32x4 kk2_0 = mfma16(WK[0], hf2_0, ZERO4); SB;
        const float om3_0 = od3[0] * rl3; SB;
        const float rl0 = __builtin_amdgcn_rcpf(od0[2]); SB;
        const f32x4 xx3_1 = mfma16(WX[1], hf3_1, xx3_0); SB;
        const float om0_2 = oc0[0] * rl0; SB;
        const float om0_4 = oc0[2] * rl0; SB;
        const f32x4 v13_1 = mfma16(hf3_1, WV1[1], v13_0); SB;
        const float om3_3 = oc3[1] * rl3; SB;
        const float om0_3 = oc0[1] * rl0; SB;
        const f32x4 kk2_1 = mfma16(WK[1], hf2_1, kk2_0); SB;
        const float om3_1 = od3[1] * rl3; SB;
        const float om1_2 = oc1[0] * rl1; SB;
        const f32x4 v13_2 = mfma16(hf3_2, WV1[2], v13_1); SB;
        const float om0_0 = od0[0] * rl0; SB;
        const float om0_1 = od0[1] * rl0; SB;
        const f32x4 xx3_2 = mfma16(WX[2], hf3_2, xx3_1); SB;
        const unsigned ow0_0 = pk2(om0_0, om0_1); SB;
        const float om1_5 = oc1[3] * rl1; SB;
        const f32x4 kk2_2 = mfma16(WK[2], hf2_2, kk2_1); SB;
        *(unsigned*)(smem + ow + OOFF + 0 + 2048) = ow0_0; SB;
        const unsigned ow1_2 = pk2(om1_4, om1_5); SB;
        const float om3_4 = oc3[2] * rl3; SB;
        const unsigned ow0_1 = pk2(om0_2, om0_3); SB;
        const float om3_5 = oc3[3] * rl3; SB;
        const float om1_3 = oc1[1] * rl1; SB;
        const unsigned ow1_1 = pk2(om1_2, om1_3); SB;
        const float om0_5 = oc0[3] * rl0; SB;
        const unsigned ow0_2 = pk2(om0_4, om0_5); SB;
        const float om3_2 = oc3[0] * rl3; SB;
        const f32x4 v12_0 = mfma16(hf2_0, WV1[0], cV1); SB;
        const unsigned ow3_1 = pk2(om3_2, om3_3); SB;
        const unsigned ow3_2 = pk2(om3_4, om3_5); SB;
        const f32x4 q2_0 = mfma16(WQ[0], hf2_0, ZERO4); SB;
        *(unsigned*)(smem + owl + 48) = ow3_1; *(unsigned*)(smem + owl + 48 + 1024) = ow3_2; SB;
        const f32x4 xx1_0 = mfma16(WX[0], hf1_0, ZERO4); SB;
        const unsigned ow3_0 = pk2(om3_0, om3_1); SB;
        const unsigned xc3 = pk2(xx3_2[2], xx3_2[3]); SB;
        const f32x4 xx1_1 = mfma16(WX[1], hf1_1, xx1_0); SB;
        *(unsigned*)(smem + owl + 48 + 2048) = ow3_0; SB;
        const f32x4 v12_1 = mfma16(hf2_1, WV1[1], v12_0); SB;
        *(unsigned*)(smem + ow + OOFF + 3072 + 2048) = ow1_0; SB;
        const f32x4 xx1_2 = mfma16(WX[2], hf1_2, xx1_1); SB;
        *(unsigned*)(smem + ow + OOFF + 0) = ow0_1; *(unsigned*)(smem + ow + OOFF + 0 + 1024) = ow0_2; SB;
        const f32x4 kk1_0 = mfma16(WK[0], hf1_0, ZERO4); SB;
        const unsigned xa3 = pk2(xx3_2[0], xx3_2[1]); SB;
        const unsigned kf2_1 = pk2(kk2_2[2], kk2_2[3]); SB;
        const f32x4 xx2_0 = mfma16(WX[0], hf2_0, ZERO4); SB;
        const auto sw3 = __builtin_amdgcn_permlane32_swap(xa3, xc3, false, false); SB;
        const f32x4 kk1_1 = mfma16(WK[1], hf1_1, kk1_0); SB;
        const unsigned kf2_0 = pk2(kk2_2[0], kk2_2[1]); SB;
        const unsigned xa1 = pk2(xx1_2[0], xx1_2[1]); SB;
        const f32x4 q2_1 = mfma16(WQ[1], hf2_1, q2_0); SB;
        *(unsigned*)(smem + ow + OOFF + 3072) = ow1_1; *(unsigned*)(smem + ow + OOFF + 3072 + 1024) = ow1_2; SB;
        const f32x4 kk3_0 = mfma16(WK[0], hf3_0, ZERO4); SB;
        const unsigned xc1 = pk2(xx1_2[2], xx1_2[3]); SB;
        VB1[3] = pk2(v13_2[2], v13_2[3]); SB;
        const f32x4 xx0_0 = mfma16(WX[0], hf0_0, ZERO4); SB;
        const auto sw1 = __builtin_amdgcn_permlane32_swap(xa1, xc1, false, false); SB;
        const f32x4 xx0_1 = mfma16(WX[1], hf0_1, xx0_0); SB;
        VB1[2] = pk2(v13_2[0], v13_2[1]); SB;
        chk = max(max(chk, fbits(od0[2])), fbits(rl0)); SB;
        const f32x4 v03_0 = mfma16(hf3_0, WV0[0], ZERO4); SB;
        chk = max(max(chk, fbits(od1[2])), fbits(rl1)); SB;
        chk = max(max(chk, fbits(od2[2])), fbits(rl2)); SB;
        const f32x4 q3_0 = mfma16(WQ[0], hf3_0, ZERO4); SB;
        chk = max(max(chk, fbits(od3[2])), fbits(rl3)); SB;
        const f32x4 q0_0 = mfma16(WQ[0], hf0_0, ZERO4); SB;
        const f32x4 v01_0 = mfma16(hf1_0, WV0[0], ZERO4); SB;
        const f32x4 v01_1 = mfma16(hf1_1, WV0[1], v01_0); SB;
        const f32x4 v02_0 = mfma16(hf2_0, WV0[0], ZERO4); SB;
        const f32x4 q1_0 = mfma16(WQ[0], hf1_0, ZERO4); SB;
        const f32x4 q1_1 = mfma16(WQ[1], hf1_1, q1_0); SB;
        const f32x4 kk3_1 = mfma16(WK[1], hf3_1, kk3_0); SB;
        const f32x4 v02_1 = mfma16(hf2_1, WV0[1], v02_0); SB;
        const f32x4 v10_0 = mfma16(hf0_0, WV1[0], cV1); SB;
        const f32x4 v00_0 = mfma16(hf0_0, WV0[0], ZERO4); SB;
        const f32x4 q3_1 = mfma16(WQ[1], hf3_1, q3_0); SB;
        const f32x4 v10_1 = mfma16(hf0_1, WV1[1], v10_0); SB;
        const f32x4 v00_1 = mfma16(hf0_1, WV0[1], v00_0); SB;
        const f32x4 xx2_1 = mfma16(WX[1], hf2_1, xx2_0); SB;
        const f32x4 v11_0 = mfma16(hf1_0, WV1[0], cV1); SB;
        const f32x4 kk0_0 = mfma16(WK[0], hf0_0, ZERO4); SB;
        const f32x4 v11_1 = mfma16(hf1_1, WV1[1], v11_0); SB;
        const f32x4 xx2_2 = mfma16(WX[2], hf2_2, xx2_1); SB;
        const f32x4 q0_1 = mfma16(WQ[1], hf0_1, q0_0); SB;
        const f32x4 v03_1 = mfma16(hf3_1, WV0[1], v03_0); SB;
        const f32x4 kk0_1 = mfma16(WK[1], hf0_1, kk0_0); SB;
        const f32x4 kk3_2 = mfma16(WK[2], hf3_2, kk3_1); SB;
        const f32x4 xx0_2 = mfma16(WX[2], hf0_2, xx0_1); SB;
        const f32x4 v02_2 = mfma16(hf2_2, WV0[2], v02_1); SB;
        const unsigned xc2 = pk2(xx2_2[2], xx2_2[3]); SB;
        const f32x4 q2_2 = mfma16(WQ[2], hf2_2, q2_1); SB;
        const unsigned xa2 = pk2(xx2_2[0], xx2_2[1]); SB;
        const f32x4 v01_2 = mfma16(hf1_2, WV0[2], v01_1); SB;
        const unsigned kf3_0 = pk2(kk3_2[0], kk3_2[1]); SB;
        const unsigned xc0 = pk2(xx0_2[2], xx0_2[3]); SB;
        const f32x4 kk1_2 = mfma16(WK[2], hf1_2, kk1_1); SB;
        const unsigned xa0 = pk2(xx0_2[0], xx0_2[1]); SB;
        const auto sw2 = __builtin_amdgcn_permlane32_swap(xa2, xc2, false, false); SB;
        Kf2[0] = kf2_0; Kf2[1] = kf2_1; Kf2[2] = sw2[1];
        const f32x4 v00_2 = mfma16(hf0_2, WV0[2], v00_1); SB;
        const auto sw0 = __builtin_amdgcn_permlane32_swap(xa0, xc0, false, false); SB;
        const f32x4 v11_2 = mfma16(hf1_2, WV1[2], v11_1); SB;
        VA0[3] = pk2(v01_2[2], v01_2[3]); SB;
        const unsigned qf2_0 = pk2(q2_2[0], q2_2[1]); SB;
        const f32x4 kk0_2 = mfma16(WK[2], hf0_2, kk0_1); SB;
        const unsigned kf1_0 = pk2(kk1_2[0], kk1_2[1]); SB;
        const unsigned qf2_1 = pk2(q2_2[2], q2_2[3]); SB;
        Qf2[0] = qf2_0; Qf2[1] = qf2_1; Qf2[2] = sw2[0];
        const f32x4 v12_2 = mfma16(hf2_2, WV1[2], v12_1); SB;
        VA1[1] = pk2(v02_2[2], v02_2[3]); SB;
        const f32x4 q0_2 = mfma16(WQ[2], hf0_2, q0_1); SB;
        VB0[3] = pk2(v11_2[2], v11_2[3]); SB;
        VB0[2] = pk2(v11_2[0], v11_2[1]); SB;
        const f32x4 v10_2 = mfma16(hf0_2, WV1[2], v10_1); SB;
        const unsigned kf0_1 = pk2(kk0_2[2], kk0_2[3]); SB;
        VA0[2] = pk2(v01_2[0], v01_2[1]); SB;
        const f32x4 q3_2 = mfma16(WQ[2], hf3_2, q3_1); SB;
        VB1[0] = pk2(v12_2[0], v12_2[1]); SB;
        VB1[1] = pk2(v12_2[2], v12_2[3]); SB;
        const f32x4 q1_2 = mfma16(WQ[2], hf1_2, q1_1); SB;
        VA1[0] = pk2(v02_2[0], v02_2[1]); SB;
        const unsigned qf0_1 = pk2(q0_2[2], q0_2[3]); SB;
        const unsigned kf3_1 = pk2(kk3_2[2], kk3_2[3]); SB;
        Kf3[0] = kf3_0; Kf3[1] = kf3_1; Kf3[2] = sw3[1];
        VB0[0] = pk2(v10_2[0], v10_2[1]); SB;
        VB0[1] = pk2(v10_2[2], v10_2[3]); SB;
        const unsigned qf0_0 = pk2(q0_2[0], q0_2[1]); SB;
        Qf0[0] = qf0_0; Qf0[1] = qf0_1; Qf0[2] = sw0[0];
        const f32x4 v03_2 = mfma16(hf3_2, WV0[2], v03_1); SB;
        const unsigned qf3_1 = pk2(q3_2[2], q3_2[3]); SB;
        const unsigned qf3_0 = pk2(q3_2[0], q3_2[1]); SB;
        Qf3[0] = qf3_0; Qf3[1] = qf3_1; Qf3[2] = sw3[0];
        VA0[0] = pk2(v00_2[0], v00_2[1]); SB;
        const unsigned qf1_1 = pk2(q1_2[2], q1_2[3]); SB;
        const unsigned kf1_1 = pk2(kk1_2[2], kk1_2[3]); SB;
        Kf1[0] = kf1_0; Kf1[1] = kf1_1; Kf1[2] = sw1[1];
        VA0[1] = pk2(v00_2[2], v00_2[3]); SB;
        VA1[2] = pk2(v03_2[0], v03_2[1]); SB;
        VA1[3] = pk2(v03_2[2], v03_2[3]); SB;
        const unsigned qf1_0 = pk2(q1_2[0], q1_2[1]); SB;
        Qf1[0] = qf1_0; Qf1[1] = qf1_1; Qf1[2] = sw1[0];
        const unsigned kf0_0 = pk2(kk0_2[0], kk0_2[1]); SB;
        Kf0[0] = kf0_0; Kf0[1] = kf0_1; Kf0[2] = sw0[1];
      }
      if constexpr (!TAIL) asm volatile("" :: "v"(Qf0), "v"(Qf1), "v"(Qf2), "v"(Qf3), "v"(Kf0), "v"(Kf1), "v"(Kf2), "v"(Kf3), "v"(VA0), "v"(VA1), "v"(VB0), "v"(VB1));
      if (__builtin_expect((__builtin_amdgcn_ballot_w64(chk >= safe_thr) != 0ull), 0)) {
        const unsigned hcur = V == 3 ? hl ^ (unsigned)(HL0 ^ HL1) : hl ^ (unsigned)(HL0 ^ HL1);
        u32x4 kf[4], va[2], vb[2];
#pragma unroll
        for (int T = 0; T < 4; ++T) {
          f32x4 ak = ZERO4, ax = ZERO4, a0 = ZERO4, a1 = cV1;
#pragma unroll
          for (int k = 0; k < 3; ++k) {
            const u32x4 hf = T < 3 ? lds128(smem, hb + hslot(V) + T * 3072 + k * 1024) : lds128(smem, hcur + k * 1024);
            ak = mfma16(WK[k], hf, ak); ax = mfma16(WX[k], hf, ax);
            a0 = mfma16(hf, WV0[k], a0); a1 = mfma16(hf, WV1[k], a1);
            SB;
          }
          const auto sw = __builtin_amdgcn_permlane32_swap(pk2(ax[0], ax[1]), pk2(ax[2], ax[3]), false, false);
          kf[T] = u32x4{pk2(ak[0], ak[1]), pk2(ak[2], ak[3]), sw[1], sw[1] & zu};
          va[T >> 1][2 * (T & 1)] = pk2(a0[0], a0[1]); va[T >> 1][2 * (T & 1) + 1] = pk2(a0[2], a0[3]);
          vb[T >> 1][2 * (T & 1)] = pk2(a1[0], a1[1]); vb[T >> 1][2 * (T & 1) + 1] = pk2(a1[2], a1[3]);
          SB;
        }
#pragma unroll
        for (int T = 0; T < 4; ++T) {
          f32x4 aq = ZERO4, ax = ZERO4;
#pragma unroll
          for (int k = 0; k < 3; ++k) {
            const u32x4 hf = T < 3 ? lds128(smem, hb + hslot(V) + T * 3072 + k * 1024) : lds128(smem, hcur + k * 1024);
            aq = mfma16(WQ[k], hf, aq); ax = mfma16(WX[k], hf, ax);
            SB;
          }
          const auto sw = __builtin_amdgcn_permlane32_swap(pk2(ax[0], ax[1]), pk2(ax[2], ax[3]), false, false);
          const u32x4 q = u32x4{pk2(aq[0], aq[1]), pk2(aq[2], aq[3]), sw[0], sw[0] & zu};
          SB;
          f32x4 s0 = mfma16(kf[0], q, ZERO4);
          f32x4 s1 = mfma16(kf[1], q, ZERO4);
          f32x4 s2 = mfma16(kf[2], q, ZERO4);
          f32x4 s3 = mfma16(kf[3], q, cS3);
          float m = fmaxf(fmaxf(fmaxf(s0[0], s0[1]), fmaxf(s0[2], s0[3])), fmaxf(fmaxf(s1[0], s1[1]), fmaxf(s1[2], s1[3])));
          m = fmaxf(m, fmaxf(fmaxf(fmaxf(s2[0], s2[1]), fmaxf(s2[2], s2[3])), s3[V]));
          {
            const auto r16 = __builtin_amdgcn_permlane16_swap(fbits(m), fbits(m), false, false);
            m = fmaxf(__builtin_bit_cast(float, (unsigned)r16[0]), __builtin_bit_cast(float, (unsigned)r16[1]));
            const auto r32 = __builtin_amdgcn_permlane32_swap(fbits(m), fbits(m), false, false);
            m = fmaxf(__builtin_bit_cast(float, (unsigned)r32[0]), __builtin_bit_cast(float, (unsigned)r32[1]));
          }
          u32x4 p0, p1;
          p0[0] = pk2(__builtin_amdgcn_exp2f(s0[0] - m), __builtin_amdgcn_exp2f(s0[1] - m));
          p0[1] = pk2(__builtin_amdgcn_exp2f(s0[2] - m), __builtin_amdgcn_exp2f(s0[3] - m));
          p0[2] = pk2(__builtin_amdgcn_exp2f(s1[0] - m), __builtin_amdgcn_exp2f(s1[1] - m));
          p0[3] = pk2(__builtin_amdgcn_exp2f(s1[2] - m), __builtin_amdgcn_exp2f(s1[3] - m));
          p1[0] = pk2(__builtin_amdgcn_exp2f(s2[0] - m), __builtin_amdgcn_exp2f(s2[1] - m));
          p1[1] = pk2(__builtin_amdgcn_exp2f(s2[2] - m), __builtin_amdgcn_exp2f(s2[3] - m));
          const float e3 = __builtin_amdgcn_exp2f(s3[V] - m);
          const unsigned pe = (V & 1) ? pk2(0.0f, e3) : pk2(e3, 0.0f);
          const unsigned pz = p0[0] & zu;
          p1[2] = V < 2 ? pe : pz;
          p1[3] = V < 2 ? pz : pe;
          f32x4 o0 = mfma16(va[0], p0, ZERO4);
          f32x4 o1 = mfma16(vb[0], p0, ZERO4);
          o0 = mfma16(va[1], p1, o0);
          o1 = mfma16(vb[1], p1, o1);
          float rl = __builtin_amdgcn_rcpf(o1[2]);
          if (T < 3) {
            char* op = smem + ow + OOFF + T * 3072;
            *(unsigned*)(op) = pk2(o0[0] * rl, o0[1] * rl);
            *(unsigned*)(op + 1024) = pk2(o0[2] * rl, o0[3] * rl);
            *(unsigned*)(op + 2048) = pk2(o1[0] * rl, o1[1] * rl);
          } else if ((t & 3) == V) {
            char* op = smem + owl + V * 16;
            *(unsigned*)(op) = pk2(o0[0] * rl, o0[1] * rl);
            *(unsigned*)(op + 1024) = pk2(o0[2] * rl, o0[3] * rl);
            *(unsigned*)(op + 2048) = pk2(o1[0] * rl, o1[1] * rl);
          }
          SB;
        }
      }
      if constexpr (V == 3) {
        hl ^= (unsigned)(HL0 ^ HL1);
        owl ^= (unsigned)(OL ^ (OL + 3072));
      }
      STAMP(20 + V);
      BAR_LGKM();
      STAMP(24 + V);
    };
#pragma nounroll
    for (int n = 0; n < 20; n += 4) {
      stepB(IC<0>{}, IC<0>{}); stepB(IC<1>{}, IC<0>{}); stepB(IC<2>{}, IC<0>{});
      if (n + 4 < nwin) stepB(IC<3>{}, IC<0>{});
    }
    __builtin_amdgcn_s_setprio(TPB);
    if (nwin & 1) stepB(IC<0>{}, IC<1>{}); else stepB(IC<3>{}, IC<1>{});
    TSLOUT();
    STAMP(10);
  }
}

extern "C" void kernel_launch(void* const* d_in, const int* in_sizes, int n_in,
                              void* d_out, int out_size, void* d_ws, size_t ws_size,
                              hipStream_t stream) {
  const unsigned short* x = (const unsigned short*)d_in[0];
  const unsigned short* dw = (const unsigned short*)d_in[1];
  const unsigned short* wqkv = (const unsigned short*)d_in[2];
  const unsigned short* wo = (const unsigned short*)d_in[3];
  davit_win_attn_kernel<<<NBLK, NTHR, 0, stream>>>(x, dw, wqkv, wo, (unsigned short*)d_out
  );
}
```
